```python
import torch

def _softmax(x, axis=-1):
    return torch.softmax(x, dim=axis)
def _gelu(x):
    return torch.nn.functional.gelu(x, approximate="tanh")
def _silu(x):
    return torch.nn.functional.silu(x)
def _sigmoid(x):
    return torch.sigmoid(x)
def _layernorm(x, axis=-1, eps=1e-5):
    m = x.mean(dim=axis, keepdim=True)
    v = x.var(dim=axis, keepdim=True, unbiased=False)
    return (x - m) / torch.sqrt(v + eps)
def _scatter_add(tgt, idx, src, axis=0):
    return tgt.clone().index_add_(axis, idx, src)
def _segment_sum(src, seg, n):
    out = torch.zeros((n,) + src.shape[1:], dtype=src.dtype)
    return out.index_add_(0, seg, src)
def _pad(x, pw):
    flat = tuple(v for pair in reversed(pw) for v in pair)
    return torch.nn.functional.pad(x, flat)

kernel_name = 'interleave_layers__grouped_kv_mqa__sliding_local_dilated__prompt'

def setup_inputs():

    _w = (lambda a: (lambda t: t.to(torch.bfloat16) if t.is_floating_point() else t)(torch.as_tensor(a)))
    def _A_setup():
        B, S, D, H = 1, 2048, 576, 32
        Dh = D // H
        return {
            "x": _w((lambda *s: torch.randn(*s, dtype=torch.bfloat16) * 0.05)(B, S, D)),
            "wq": _w((lambda *s: torch.randn(*s, dtype=torch.bfloat16) * 0.05)(D, H * Dh)),
            "wk": _w((lambda *s: torch.randn(*s, dtype=torch.bfloat16) * 0.05)(D, Dh)),
            "wv": _w((lambda *s: torch.randn(*s, dtype=torch.bfloat16) * 0.05)(D, Dh)),
            "wo": _w((lambda *s: torch.randn(*s, dtype=torch.bfloat16) * 0.05)(H * Dh, D)),
        }
    _A_in = _A_setup()

    def _B_setup():
        B, S, D = 1, 2048, 576
        return {
            "x": _w((lambda *s: torch.randn(*s, dtype=torch.bfloat16) * 0.05)(B, S, D)),
            "wqkv": _w((lambda *s: torch.randn(*s, dtype=torch.bfloat16) * 0.05)(D, 3 * D)),
            "wo": _w((lambda *s: torch.randn(*s, dtype=torch.bfloat16) * 0.05)(D, D)),
        }
    _B_in = _B_setup()

    return {
        "A_x": _A_in["x"],
        "A_wq": _A_in["wq"],
        "A_wk": _A_in["wk"],
        "A_wv": _A_in["wv"],
        "A_wo": _A_in["wo"],
        "B_x": _B_in["x"],
        "B_wqkv": _B_in["wqkv"],
        "B_wo": _B_in["wo"],
    }

def reference(A_x, A_wq, A_wk, A_wv, A_wo, B_x, B_wqkv, B_wo):
    def _A_ref(x, wq, wk, wv, wo):
        B, S, D = x.shape
        H = 32
        Dh = D // H
        q = (lambda x, *d: x.permute(*d))((x @ wq).reshape(B, S, H, Dh), 0, 2, 1, 3)
        k = x @ wk
        v = x @ wv
        scores = torch.einsum("bhqd,bkd->bhqk", q, k) * (Dh ** -0.5)
        p = _softmax(scores, axis=-1)
        o = torch.einsum("bhqk,bkd->bhqd", p, v)
        return (lambda x, *d: x.permute(*d))(o, 0, 2, 1, 3).reshape(B, S, H * Dh) @ wo

    def _B_ref(x, wqkv, wo):
        B, S, D = x.shape
        H = 32; Dh = D // H
        W, dil = 8, 2
        qkv = (lambda x, *d: x.permute(*d))((x @ wqkv).reshape(B, S, 3, H, Dh), 2, 0, 3, 1, 4)
        q, k, v = qkv[0], qkv[1], qkv[2]
        scale = Dh ** -0.5
        pos = torch.arange(S)
        off = torch.arange(W) * dil
        idx = pos[:, None] - off[None, :]
        mask = idx >= 0
        idx_c = torch.where(mask, idx, 0).reshape(-1)
        kg = k[:, :, idx_c, :].reshape(B, H, S, W, Dh)
        vg = v[:, :, idx_c, :].reshape(B, H, S, W, Dh)
        logits = torch.einsum("bhsd,bhswd->bhsw", q, kg) * scale
        logits = torch.where(mask[None, None, :, :], logits, -1e9)
        attn = _softmax(logits, axis=-1)
        o = torch.einsum("bhsw,bhswd->bhsd", attn, vg)
        return (lambda x, *d: x.permute(*d))(o, 0, 2, 1, 3).reshape(B, S, D) @ wo

    x = A_x
    x = x + _A_ref(_layernorm(x, axis=-1), A_wq, A_wk, A_wv, A_wo)
    x = x + _B_ref(_layernorm(x, axis=-1), B_wqkv, B_wo)
    x = x + _A_ref(_layernorm(x, axis=-1), A_wq, A_wk, A_wv, A_wo)
    x = x + _B_ref(_layernorm(x, axis=-1), B_wqkv, B_wo)
    return x
```

```cpp
#include <hip/hip_runtime.h>
#include <hip/hip_bf16.h>
#include <stdint.h>

typedef __attribute__((ext_vector_type(8))) short bf16x8;
typedef __attribute__((ext_vector_type(4))) float f32x4;
typedef __attribute__((ext_vector_type(2))) float f32x2;
typedef __attribute__((ext_vector_type(16))) float f32x16;
typedef __attribute__((ext_vector_type(4))) unsigned u32x4;
typedef __attribute__((ext_vector_type(2))) __bf16 bf16x2_t;
typedef unsigned short u16;

constexpr int S_ = 2048, D_ = 576;
constexpr int KS = 18;
constexpr int LDA = 1168;
constexpr int PANEL_CH = 2 * KS * 64;

constexpr size_t PANEL_B = (size_t)PANEL_CH * 16;
constexpr size_t OFF_WQKVA = 0;
constexpr size_t OFF_WOA = OFF_WQKVA + 20 * PANEL_B;
constexpr size_t OFF_WQKVB = OFF_WOA + 18 * PANEL_B;
constexpr size_t OFF_WOB = OFF_WQKVB + 56 * PANEL_B;
constexpr size_t ACT_B = (size_t)S_ * D_ * 2;
constexpr size_t OFF_Q = OFF_WOB + 18 * PANEL_B;
constexpr size_t OFF_O = OFF_Q + ACT_B;
constexpr size_t OFF_X1 = OFF_O + ACT_B;
constexpr size_t OFF_X2 = OFF_X1 + ACT_B;
constexpr size_t OFF_X3 = OFF_X2 + ACT_B;
constexpr size_t OFF_KV = OFF_X3 + ACT_B;
constexpr int KV_KLO = 0, KV_KHI = 65536, KV_VT = 65536 + 8192;
constexpr int KV_BYTES = KV_VT + 64 * 4 * 19 * 16;

__device__ __forceinline__ unsigned pk2(float a, float b) {
  f32x2 v = {a, b};
  bf16x2_t r = __builtin_convertvector(v, bf16x2_t);
  return __builtin_bit_cast(unsigned, r);
}
__device__ __forceinline__ float bflo(unsigned u) { return __uint_as_float(u << 16); }
__device__ __forceinline__ float bfhi(unsigned u) { return __uint_as_float(u & 0xffff0000u); }
__device__ __forceinline__ float rbf(float a) { return bflo(pk2(a, 0.f) & 0xffffu); }

__global__ __launch_bounds__(256) void prep_kernel(const u16* __restrict__ wq, const u16* __restrict__ wk,
                                                   const u16* __restrict__ wv, const u16* __restrict__ woa,
                                                   const u16* __restrict__ wqkv, const u16* __restrict__ wob,
                                                   u32x4* __restrict__ out) {
  int id = blockIdx.x * 256 + threadIdx.x;
  int panel = id / PANEL_CH, rem = id % PANEL_CH;
  int t = rem / (KS * 64), ks = (rem / 64) % KS, lane = rem & 63;
  int i = lane & 15, c = 8 * (i >> 2) + 4 * t + (i & 3);
  int k0 = 32 * ks + 8 * (lane >> 4);
  const u16* src = nullptr; int ld = 0, col = 0;
  if (panel < 18) { src = wq; ld = 576; col = 32 * panel + c; }
  else if (panel < 20) { if (c < 18) { src = (panel == 18) ? wk : wv; ld = 18; col = c; } }
  else if (panel < 38) { src = woa; ld = 576; col = 32 * (panel - 20) + c; }
  else if (panel < 94) {
    int p = panel - 38, g = p / 7, cc = 32 * (p % 7) + c;
    if (cc < 216) { src = wqkv; ld = 1728; col = (cc / 72) * 576 + g * 72 + (cc % 72); }
  } else { src = wob; ld = 576; col = 32 * (panel - 94) + c; }
  u32x4 v = {0u, 0u, 0u, 0u};
  if (src) {
#pragma unroll
    for (int j = 0; j < 4; ++j) {
      unsigned lo = src[(size_t)(k0 + 2 * j) * ld + col];
      unsigned hi = src[(size_t)(k0 + 2 * j + 1) * ld + col];
      v[j] = lo | (hi << 16);
    }
  }
  out[id] = v;
}

template <bool LN>
__device__ __forceinline__ void stage8(const u16* __restrict__ src, int grow0, char* lds, int lr0, int lane) {
  int rr = lane >> 3, c = lane & 7;
  int grow = grow0 + rr; grow = grow < 0 ? 0 : grow;
  const u32x4* p = (const u32x4*)(src + (size_t)grow * D_) + c;
  u32x4 v[9];
#pragma unroll
  for (int i = 0; i < 9; ++i) v[i] = p[8 * i];
  if (LN) {
    float s = 0.f;
#pragma unroll
    for (int i = 0; i < 9; ++i)
#pragma unroll
      for (int j = 0; j < 4; ++j) s += bflo(v[i][j]) + bfhi(v[i][j]);
    s += __shfl_xor(s, 1); s += __shfl_xor(s, 2); s += __shfl_xor(s, 4);
    float mean = s * (1.f / 576.f);
#pragma unroll
    for (int i = 0; i < 9; ++i) asm volatile("" : "+v"(v[i]));
    float q = 0.f;
#pragma unroll
    for (int i = 0; i < 9; ++i)
#pragma unroll
      for (int j = 0; j < 4; ++j) {
        float a = bflo(v[i][j]) - mean, b = bfhi(v[i][j]) - mean;
        q += a * a + b * b;
      }
    q += __shfl_xor(q, 1); q += __shfl_xor(q, 2); q += __shfl_xor(q, 4);
    float rstd = rsqrtf(q * (1.f / 576.f) + 1e-5f);
#pragma unroll
    for (int i = 0; i < 9; ++i) asm volatile("" : "+v"(v[i]));
#pragma unroll
    for (int i = 0; i < 9; ++i)
#pragma unroll
      for (int j = 0; j < 4; ++j)
        v[i][j] = pk2((bflo(v[i][j]) - mean) * rstd, (bfhi(v[i][j]) - mean) * rstd);
  }
  char* d = lds + (lr0 + rr) * LDA + c * 16;
#pragma unroll
  for (int i = 0; i < 9; ++i) *(u32x4*)(d + 128 * i) = v[i];
}

#define MFMA16(a, b, c) __builtin_amdgcn_mfma_f32_16x16x32_bf16(a, b, c, 0, 0, 0)
#define MFMA32(a, b, c) __builtin_amdgcn_mfma_f32_32x32x16_bf16(a, b, c, 0, 0, 0)

template <int MT>
__device__ __forceinline__ void wave_gemm(const bf16x8 (&b)[2][KS], const char* lds, int mt0, int lane,
                                          f32x4 (&acc)[MT][2]) {
  const char* ap = lds + (mt0 * 16 + (lane & 15)) * LDA + (lane >> 4) * 16;
#pragma unroll
  for (int ks = 0; ks < KS; ++ks) {
#pragma unroll
    for (int m = 0; m < MT; ++m) {
      bf16x8 a = *(const bf16x8*)(ap + m * 16 * LDA + ks * 64);
      acc[m][0] = MFMA16(b[0][ks], a, acc[m][0]);
      acc[m][1] = MFMA16(b[1][ks], a, acc[m][1]);
    }
  }
}

__device__ __forceinline__ void load_panel(const u32x4* __restrict__ wp, int panel, int lane, bf16x8 (&b)[2][KS]) {
  const u32x4* p = wp + (size_t)panel * PANEL_CH + lane;
#pragma unroll
  for (int t = 0; t < 2; ++t)
#pragma unroll
    for (int ks = 0; ks < KS; ++ks) b[t][ks] = __builtin_bit_cast(bf16x8, p[(t * KS + ks) * 64]);
}

__global__ __launch_bounds__(384) void ln_qkv_a_kernel(const u16* __restrict__ x, const u32x4* __restrict__ wp,
                                                       u16* __restrict__ q, char* __restrict__ kv) {
  extern __shared__ __attribute__((aligned(16))) char lds[];
  const int tid = threadIdx.x, lane = tid & 63, w = tid >> 6;
  const int b = blockIdx.x;
  int rt, panel;
  const int mh = w / 3, pw = w % 3;
  bool active = true;
  if (b < 192) { rt = b / 6; panel = 3 * (b % 6) + pw; }
  else { rt = b - 192; panel = 18 + pw; active = pw < 2; }
  bf16x8 bw[2][KS];
  if (active) load_panel(wp, panel, lane, bw);
  stage8<true>(x, rt * 64 + w * 8, lds, w * 8, lane);
  if (w < 2) stage8<true>(x, rt * 64 + (w + 6) * 8, lds, (w + 6) * 8, lane);
  __syncthreads();
  if (!active) return;
  f32x4 acc[2][2];
#pragma unroll
  for (int m = 0; m < 2; ++m) { acc[m][0] = (f32x4){0.f, 0.f, 0.f, 0.f}; acc[m][1] = acc[m][0]; }
  wave_gemm<2>(bw, lds, mh * 2, lane, acc);
  const int g = lane >> 4;
#pragma unroll
  for (int m = 0; m < 2; ++m) {
    const int row = rt * 64 + (mh * 2 + m) * 16 + (lane & 15);
    if (panel < 18) {
      const float c = 0.23570226039551584f * 1.4426950408889634f;
      u32x4 v;
      v[0] = pk2(acc[m][0][0] * c, acc[m][0][1] * c); v[1] = pk2(acc[m][0][2] * c, acc[m][0][3] * c);
      v[2] = pk2(acc[m][1][0] * c, acc[m][1][1] * c); v[3] = pk2(acc[m][1][2] * c, acc[m][1][3] * c);
      *(u32x4*)(q + (size_t)row * D_ + panel * 32 + g * 8) = v;
    } else if (panel == 18) {
      u32x4 v;
      v[0] = pk2(acc[m][0][0], acc[m][0][1]); v[1] = pk2(acc[m][0][2], acc[m][0][3]);
      v[2] = pk2(acc[m][1][0], acc[m][1][1]); v[3] = pk2(acc[m][1][2], acc[m][1][3]);
      if (g < 2) *(u32x4*)(kv + KV_KLO + row * 32 + g * 16) = v;
      else if (g == 2) *(unsigned*)(kv + KV_KHI + row * 4) = v[0];
    } else {
      const int T = row >> 5, kk = row & 31;
      const int s = kk >> 4, h = (kk >> 2) & 1, j = ((kk >> 3) & 1) * 4 + (kk & 3);
      u16* vb = (u16*)(kv + KV_VT + (((T * 2 + s) * 2 + h) * 19) * 16 + j * 2);
#pragma unroll
      for (int t = 0; t < 2; ++t)
#pragma unroll
        for (int r = 0; r < 4; ++r) {
          const int d = 8 * g + 4 * t + r;
          if (d < 18) vb[d * 8] = (u16)(pk2(acc[m][t][r], 0.f) & 0xffffu);
          else if (d == 18) vb[d * 8] = (u16)0x3F80;
        }
    }
  }
}

__global__ __launch_bounds__(384) void proj_res_kernel(const u16* __restrict__ o, const u32x4* __restrict__ wp,
                                                       const u16* __restrict__ xin, u16* __restrict__ xout) {
  extern __shared__ __attribute__((aligned(16))) char lds[];
  const int tid = threadIdx.x, lane = tid & 63, w = tid >> 6;
  const int b = blockIdx.x;
  const int mh = w / 3, pw = w % 3;
  const int rt = b / 6, panel = 3 * (b % 6) + pw;
  bf16x8 bw[2][KS];
  load_panel(wp, panel, lane, bw);
  stage8<false>(o, rt * 64 + w * 8, lds, w * 8, lane);
  if (w < 2) stage8<false>(o, rt * 64 + (w + 6) * 8, lds, (w + 6) * 8, lane);
  const int g = lane >> 4;
  u32x4 xr[2];
#pragma unroll
  for (int m = 0; m < 2; ++m) {
    const int row = rt * 64 + (mh * 2 + m) * 16 + (lane & 15);
    xr[m] = *(const u32x4*)(xin + (size_t)row * D_ + panel * 32 + g * 8);
  }
  __syncthreads();
  f32x4 acc[2][2];
#pragma unroll
  for (int m = 0; m < 2; ++m) { acc[m][0] = (f32x4){0.f, 0.f, 0.f, 0.f}; acc[m][1] = acc[m][0]; }
  wave_gemm<2>(bw, lds, mh * 2, lane, acc);
#pragma unroll
  for (int m = 0; m < 2; ++m) {
    const int row = rt * 64 + (mh * 2 + m) * 16 + (lane & 15);
    u32x4 v;
#pragma unroll
    for (int j = 0; j < 4; ++j) {
      const float a0 = rbf(acc[m][j >> 1][(j & 1) * 2]), a1 = rbf(acc[m][j >> 1][(j & 1) * 2 + 1]);
      v[j] = pk2(bflo(xr[m][j]) + a0, bfhi(xr[m][j]) + a1);
    }
    *(u32x4*)(xout + (size_t)row * D_ + panel * 32 + g * 8) = v;
  }
}

constexpr int QLD = 232;
__global__ __launch_bounds__(448) void ln_qkv_local_kernel(const u16* __restrict__ x, const u32x4* __restrict__ wp,
                                                           u16* __restrict__ o) {
  extern __shared__ __attribute__((aligned(16))) char lds[];
  const int tid = threadIdx.x, lane = tid & 63, w = tid >> 6;
  const int b = blockIdx.x;
  const int rt = b >> 3, hg = b & 7;
  const int r0 = rt * 64 - 16;
  bf16x8 bw[2][KS];
  load_panel(wp, hg * 7 + w, lane, bw);
  stage8<true>(x, r0 + w * 8, lds, w * 8, lane);
  if (w < 3) stage8<true>(x, r0 + (w + 7) * 8, lds, (w + 7) * 8, lane);
  __syncthreads();
  f32x4 acc[5][2];
#pragma unroll
  for (int m = 0; m < 5; ++m) { acc[m][0] = (f32x4){0.f, 0.f, 0.f, 0.f}; acc[m][1] = acc[m][0]; }
  wave_gemm<5>(bw, lds, 0, lane, acc);
  __syncthreads();
  u16* t = (u16*)lds;
  const int g = lane >> 4;
#pragma unroll
  for (int m = 0; m < 5; ++m) {
    u32x4 v;
    v[0] = pk2(acc[m][0][0], acc[m][0][1]); v[1] = pk2(acc[m][0][2], acc[m][0][3]);
    v[2] = pk2(acc[m][1][0], acc[m][1][1]); v[3] = pk2(acc[m][1][2], acc[m][1][3]);
    *(u32x4*)(t + (m * 16 + (lane & 15)) * QLD + w * 32 + g * 8) = v;
  }
  __syncthreads();
  if (tid < 256) {
    const int hh = tid >> 6, rl = tid & 63;
    const int grow = rt * 64 + rl, lrow = rl + 16;
    const unsigned* qp = (const unsigned*)(t + lrow * QLD + hh * 18);
    float qf[18];
#pragma unroll
    for (int i = 0; i < 9; ++i) { unsigned u = qp[i]; qf[2 * i] = bflo(u); qf[2 * i + 1] = bfhi(u); }
    float lg[8];
    float mx = -3.0e38f;
#pragma unroll
    for (int wi = 0; wi < 8; ++wi) {
      const unsigned* kp = (const unsigned*)(t + (lrow - 2 * wi) * QLD + 72 + hh * 18);
      float s = 0.f;
#pragma unroll
      for (int i = 0; i < 9; ++i) { unsigned u = kp[i]; s += qf[2 * i] * bflo(u) + qf[2 * i + 1] * bfhi(u); }
      s *= 0.23570226039551584f;
      s = (grow - 2 * wi >= 0) ? s : -1e9f;
      lg[wi] = s; mx = fmaxf(mx, s);
    }
    float sum = 0.f;
#pragma unroll
    for (int wi = 0; wi < 8; ++wi) { lg[wi] = __expf(lg[wi] - mx); sum += lg[wi]; }
    const float inv = 1.f / sum;
    float of[18];
#pragma unroll
    for (int i = 0; i < 18; ++i) of[i] = 0.f;
#pragma unroll
    for (int wi = 0; wi < 8; ++wi) {
      const unsigned* vp = (const unsigned*)(t + (lrow - 2 * wi) * QLD + 144 + hh * 18);
      const float p = lg[wi] * inv;
#pragma unroll
      for (int i = 0; i < 9; ++i) { unsigned u = vp[i]; of[2 * i] += p * bflo(u); of[2 * i + 1] += p * bfhi(u); }
    }
    unsigned* op = (unsigned*)(o + (size_t)grow * D_ + (hg * 4 + hh) * 18);
#pragma unroll
    for (int i = 0; i < 9; ++i) op[i] = pk2(of[2 * i], of[2 * i + 1]);
  }
}

constexpr float ABIAS = 6.0f;
__device__ __forceinline__ float xhalf_max(float m) {
  auto rr = __builtin_amdgcn_permlane32_swap(__float_as_uint(m), __float_as_uint(m), false, false);
  return fmaxf(__uint_as_float(rr[0]), __uint_as_float(rr[1]));
}
__global__ __launch_bounds__(512, 2) void attn_kernel(const u16* __restrict__ q, const u32x4* __restrict__ kv,
                                                      u16* __restrict__ o) {
  extern __shared__ __attribute__((aligned(16))) char lds[];
  const int tid = threadIdx.x, lane = tid & 63, w = tid >> 6;
  const int r = lane & 31, h = lane >> 5;
  const int qt = blockIdx.x >> 2, head = (blockIdx.x & 3) * 8 + w;
  for (int i = tid; i < KV_BYTES / 16; i += 512) ((u32x4*)lds)[i] = kv[i];
  const int qrow = qt * 32 + r;
  const unsigned* qp = (const unsigned*)(q + (size_t)qrow * D_ + head * 18);
  u32x4 qlo_u, qhi_u = {0u, 0u, 0u, 0u};
#pragma unroll
  for (int i = 0; i < 4; ++i) qlo_u[i] = qp[h * 4 + i];
  if (h == 0) qhi_u[0] = qp[8];
  const bf16x8 qlo = __builtin_bit_cast(bf16x8, qlo_u), qhi = __builtin_bit_cast(bf16x8, qhi_u);
  __syncthreads();
  const char* kloP = lds + KV_KLO + r * 32 + h * 16;
  const char* khiP = lds + KV_KHI + r * 4;
  const int dcl = r < 19 ? r : 18;
  const char* vP = lds + KV_VT + h * 304 + dcl * 16;
  f32x16 negm, acc;
#pragma unroll
  for (int i = 0; i < 16; ++i) { negm[i] = 0.f; acc[i] = 0.f; }
  {
    bf16x8 klo = *(const bf16x8*)kloP;
    u32x4 kh = {*(const unsigned*)khiP, 0u, 0u, 0u};
    f32x16 s = MFMA32(klo, qlo, negm);
    s = MFMA32(__builtin_bit_cast(bf16x8, kh), qhi, s);
    float tm = s[0];
#pragma unroll
    for (int i = 1; i < 16; ++i) tm = fmaxf(tm, s[i]);
    tm = xhalf_max(tm) + ABIAS;
#pragma unroll
    for (int i = 0; i < 16; ++i) negm[i] = -tm;
  }
  for (int t = 0; t < 64; ++t) {
    bf16x8 klo = *(const bf16x8*)(kloP + t * 1024);
    u32x4 kh = {*(const unsigned*)(khiP + t * 128), 0u, 0u, 0u};
    bf16x8 v0 = *(const bf16x8*)(vP + t * 1216);
    bf16x8 v1 = *(const bf16x8*)(vP + t * 1216 + 608);
    f32x16 s = MFMA32(klo, qlo, negm);
    s = MFMA32(__builtin_bit_cast(bf16x8, kh), qhi, s);
    u32x4 p0, p1;
#pragma unroll
    for (int i = 0; i < 4; ++i) {
      p0[i] = pk2(__builtin_amdgcn_exp2f(s[2 * i]), __builtin_amdgcn_exp2f(s[2 * i + 1]));
      p1[i] = pk2(__builtin_amdgcn_exp2f(s[8 + 2 * i]), __builtin_amdgcn_exp2f(s[8 + 2 * i + 1]));
    }
    unsigned orr = p0[0] | p0[1] | p0[2] | p0[3] | p1[0] | p1[1] | p1[2] | p1[3];
    if (__builtin_expect(__any((orr & 0x40004000u) != 0u), 0)) {
      float tm = s[0];
#pragma unroll
      for (int i = 1; i < 16; ++i) tm = fmaxf(tm, s[i]);
      tm = xhalf_max(tm);
      const float dl = fmaxf(tm + ABIAS, 0.f);
      const float al = __builtin_amdgcn_exp2f(-dl);
#pragma unroll
      for (int i = 0; i < 16; ++i) { acc[i] *= al; negm[i] -= dl; s[i] -= dl; }
#pragma unroll
      for (int i = 0; i < 4; ++i) {
        p0[i] = pk2(__builtin_amdgcn_exp2f(s[2 * i]), __builtin_amdgcn_exp2f(s[2 * i + 1]));
        p1[i] = pk2(__builtin_amdgcn_exp2f(s[8 + 2 * i]), __builtin_amdgcn_exp2f(s[8 + 2 * i + 1]));
      }
    }
    acc = MFMA32(v0, __builtin_bit_cast(bf16x8, p0), acc);
    acc = MFMA32(v1, __builtin_bit_cast(bf16x8, p1), acc);
  }
  auto rr = __builtin_amdgcn_permlane32_swap(__float_as_uint(acc[10]), __float_as_uint(acc[10]), false, false);
  const float l = __uint_as_float(rr[0]);
  const float inv = 1.f / l;
  unsigned* op = (unsigned*)(o + (size_t)qrow * D_ + head * 18);
  op[2 * h + 0] = pk2(acc[0] * inv, acc[1] * inv);
  op[2 * h + 1] = pk2(acc[2] * inv, acc[3] * inv);
  op[4 + 2 * h + 0] = pk2(acc[4] * inv, acc[5] * inv);
  op[4 + 2 * h + 1] = pk2(acc[6] * inv, acc[7] * inv);
  if (h == 0) op[8] = pk2(acc[8] * inv, acc[9] * inv);
}

extern "C" void kernel_launch(void* const* d_in, const int* in_sizes, int n_in, void* d_out, int out_size,
                              void* d_ws, size_t ws_size, hipStream_t stream) {
  const u16* x0 = (const u16*)d_in[0];
  const u16 *wq = (const u16*)d_in[1], *wk = (const u16*)d_in[2], *wv = (const u16*)d_in[3];
  const u16 *woa = (const u16*)d_in[4], *wqkv = (const u16*)d_in[6], *wob = (const u16*)d_in[7];
  char* ws = (char*)d_ws;
  u16* xs[5] = {(u16*)x0, (u16*)(ws + OFF_X1), (u16*)(ws + OFF_X2), (u16*)(ws + OFF_X3), (u16*)d_out};
  u16* qb = (u16*)(ws + OFF_Q);
  u16* ob = (u16*)(ws + OFF_O);
  char* kv = ws + OFF_KV;
  (void)hipFuncSetAttribute((const void*)attn_kernel, hipFuncAttributeMaxDynamicSharedMemorySize, KV_BYTES);
  (void)hipFuncSetAttribute((const void*)ln_qkv_a_kernel, hipFuncAttributeMaxDynamicSharedMemorySize, 64 * LDA);
  (void)hipFuncSetAttribute((const void*)proj_res_kernel, hipFuncAttributeMaxDynamicSharedMemorySize, 64 * LDA);
  (void)hipFuncSetAttribute((const void*)ln_qkv_local_kernel, hipFuncAttributeMaxDynamicSharedMemorySize, 80 * LDA);
  prep_kernel<<<112 * PANEL_CH / 256, 256, 0, stream>>>(wq, wk, wv, woa, wqkv, wob, (u32x4*)ws);
  for (int l = 0; l < 2; ++l) {
    ln_qkv_a_kernel<<<224, 384, 64 * LDA, stream>>>(xs[2 * l], (const u32x4*)(ws + OFF_WQKVA), qb, kv);
    attn_kernel<<<256, 512, KV_BYTES, stream>>>(qb, (const u32x4*)kv, ob);
    proj_res_kernel<<<192, 384, 64 * LDA, stream>>>(ob, (const u32x4*)(ws + OFF_WOA), xs[2 * l], xs[2 * l + 1]);
    ln_qkv_local_kernel<<<256, 448, 80 * LDA, stream>>>(xs[2 * l + 1], (const u32x4*)(ws + OFF_WQKVB), ob);
    proj_res_kernel<<<192, 384, 64 * LDA, stream>>>(ob, (const u32x4*)(ws + OFF_WOB), xs[2 * l + 1], xs[2 * l + 2]);
  }
}
```

```cpp
#include <hip/hip_runtime.h>
#include <hip/hip_bf16.h>
#include <stdint.h>

typedef __attribute__((ext_vector_type(8))) short bf16x8;
typedef __attribute__((ext_vector_type(4))) float f32x4;
typedef __attribute__((ext_vector_type(2))) float f32x2;
typedef __attribute__((ext_vector_type(16))) float f32x16;
typedef __attribute__((ext_vector_type(4))) unsigned u32x4;
typedef __attribute__((ext_vector_type(2))) __bf16 bf16x2_t;
typedef unsigned short u16;

constexpr int S_ = 2048, D_ = 576;
constexpr int KS = 18;
constexpr int LDA = 1168;
constexpr int PANEL_CH = 2 * KS * 64;

constexpr size_t PANEL_B = (size_t)PANEL_CH * 16;
constexpr size_t OFF_WQKVA = 0;
constexpr size_t OFF_WOA = OFF_WQKVA + 20 * PANEL_B;
constexpr size_t OFF_WQKVB = OFF_WOA + 18 * PANEL_B;
constexpr size_t OFF_WOB = OFF_WQKVB + 56 * PANEL_B;
constexpr size_t ACT_B = (size_t)S_ * D_ * 2;
constexpr size_t OFF_Q = OFF_WOB + 18 * PANEL_B;
constexpr size_t OFF_O = OFF_Q + ACT_B;
constexpr size_t OFF_X1 = OFF_O + ACT_B;
constexpr size_t OFF_X2 = OFF_X1 + ACT_B;
constexpr size_t OFF_X3 = OFF_X2 + ACT_B;
constexpr size_t OFF_KV = OFF_X3 + ACT_B;
constexpr int KV_KLO = 0, KV_KHI = 65536, KV_VT = 65536 + 8192;
constexpr int KV_BYTES = KV_VT + 64 * 4 * 19 * 16;

#define STAMP(K, I)
__device__ __forceinline__ unsigned pk2(float a, float b) {
  f32x2 v = {a, b};
  bf16x2_t r = __builtin_convertvector(v, bf16x2_t);
  return __builtin_bit_cast(unsigned, r);
}
__device__ __forceinline__ float bflo(unsigned u) { return __uint_as_float(u << 16); }
__device__ __forceinline__ float bfhi(unsigned u) { return __uint_as_float(u & 0xffff0000u); }
__device__ __forceinline__ float rbf(float a) { return bflo(pk2(a, 0.f) & 0xffffu); }

__global__ __launch_bounds__(256) void prep_kernel(const u16* __restrict__ wq, const u16* __restrict__ wk,
                                                   const u16* __restrict__ wv, const u16* __restrict__ woa,
                                                   const u16* __restrict__ wqkv, const u16* __restrict__ wob,
                                                   u32x4* __restrict__ out) {
  int id = blockIdx.x * 256 + threadIdx.x;
  int panel = id / PANEL_CH, rem = id % PANEL_CH;
  int t = rem / (KS * 64), ks = (rem / 64) % KS, lane = rem & 63;
  int i = lane & 15, c = 8 * (i >> 2) + 4 * t + (i & 3);
  int k0 = 32 * ks + 8 * (lane >> 4);
  const u16* src = nullptr; int ld = 0, col = 0;
  if (panel < 18) { src = wq; ld = 576; col = 32 * panel + c; }
  else if (panel < 20) { if (c < 18) { src = (panel == 18) ? wk : wv; ld = 18; col = c; } }
  else if (panel < 38) { src = woa; ld = 576; col = 32 * (panel - 20) + c; }
  else if (panel < 94) {
    int p = panel - 38, g = p / 7, cc = 32 * (p % 7) + c;
    if (cc < 216) { src = wqkv; ld = 1728; col = (cc / 72) * 576 + g * 72 + (cc % 72); }
  } else { src = wob; ld = 576; col = 32 * (panel - 94) + c; }
  u32x4 v = {0u, 0u, 0u, 0u};
  if (src) {
#pragma unroll
    for (int j = 0; j < 4; ++j) {
      unsigned lo = src[(size_t)(k0 + 2 * j) * ld + col];
      unsigned hi = src[(size_t)(k0 + 2 * j + 1) * ld + col];
      v[j] = lo | (hi << 16);
    }
  }
  out[id] = v;
}

template <bool LN>
__device__ __forceinline__ void stage8(const u16* __restrict__ src, int grow0, char* lds, int lr0, int lane) {
  int rr = lane >> 3, c = lane & 7;
  int grow = grow0 + rr; grow = grow < 0 ? 0 : grow;
  const u32x4* p = (const u32x4*)(src + (size_t)grow * D_) + c;
  u32x4 v[9];
#pragma unroll
  for (int i = 0; i < 9; ++i) v[i] = p[8 * i];
  if (LN) {
    float s = 0.f;
#pragma unroll
    for (int i = 0; i < 9; ++i)
#pragma unroll
      for (int j = 0; j < 4; ++j) s += bflo(v[i][j]) + bfhi(v[i][j]);
    s += __shfl_xor(s, 1); s += __shfl_xor(s, 2); s += __shfl_xor(s, 4);
    float mean = s * (1.f / 576.f);
#pragma unroll
    for (int i = 0; i < 9; ++i) asm volatile("" : "+v"(v[i]));
    float q = 0.f;
#pragma unroll
    for (int i = 0; i < 9; ++i)
#pragma unroll
      for (int j = 0; j < 4; ++j) {
        float a = bflo(v[i][j]) - mean, b = bfhi(v[i][j]) - mean;
        q += a * a + b * b;
      }
    q += __shfl_xor(q, 1); q += __shfl_xor(q, 2); q += __shfl_xor(q, 4);
    float rstd = rsqrtf(q * (1.f / 576.f) + 1e-5f);
#pragma unroll
    for (int i = 0; i < 9; ++i) asm volatile("" : "+v"(v[i]));
#pragma unroll
    for (int i = 0; i < 9; ++i)
#pragma unroll
      for (int j = 0; j < 4; ++j)
        v[i][j] = pk2((bflo(v[i][j]) - mean) * rstd, (bfhi(v[i][j]) - mean) * rstd);
  }
  char* d = lds + (lr0 + rr) * LDA + c * 16;
#pragma unroll
  for (int i = 0; i < 9; ++i) *(u32x4*)(d + 128 * i) = v[i];
}

#define MFMA16(a, b, c) __builtin_amdgcn_mfma_f32_16x16x32_bf16(a, b, c, 0, 0, 0)
#define MFMA32(a, b, c) __builtin_amdgcn_mfma_f32_32x32x16_bf16(a, b, c, 0, 0, 0)

template <int MT>
__device__ __forceinline__ void wave_gemm(const bf16x8 (&b)[2][KS], const char* lds, int mt0, int lane,
                                          f32x4 (&acc)[MT][2]) {
  const char* ap = lds + (mt0 * 16 + (lane & 15)) * LDA + (lane >> 4) * 16;
#pragma unroll
  for (int ks = 0; ks < KS; ++ks) {
#pragma unroll
    for (int m = 0; m < MT; ++m) {
      bf16x8 a = *(const bf16x8*)(ap + m * 16 * LDA + ks * 64);
      acc[m][0] = MFMA16(b[0][ks], a, acc[m][0]);
      acc[m][1] = MFMA16(b[1][ks], a, acc[m][1]);
    }
  }
}

__device__ __forceinline__ void load_panel(const u32x4* __restrict__ wp, int panel, int lane, bf16x8 (&b)[2][KS]) {
  const u32x4* p = wp + (size_t)panel * PANEL_CH + lane;
#pragma unroll
  for (int t = 0; t < 2; ++t)
#pragma unroll
    for (int ks = 0; ks < KS; ++ks) b[t][ks] = __builtin_bit_cast(bf16x8, p[(t * KS + ks) * 64]);
}

__global__ __launch_bounds__(384) void ln_qkv_a_kernel(const u16* __restrict__ x, const u32x4* __restrict__ wp,
                                                       u16* __restrict__ q, char* __restrict__ kv) {
  extern __shared__ __attribute__((aligned(16))) char lds[];
  const int tid = threadIdx.x, lane = tid & 63, w = tid >> 6;
  const int b = blockIdx.x;
  int rt, panel;
  const int mh = w / 3, pw = w % 3;
  bool active = true;
  {
    const int xcd = b & 7, idx = b >> 3;
    rt = 4 * xcd + idx / 7;
    const int cg = idx % 7;
    panel = 3 * cg + pw;
    active = panel < 20;
  }
  bf16x8 bw[2][KS];
  if (active) load_panel(wp, panel, lane, bw);
  stage8<true>(x, rt * 64 + w * 8, lds, w * 8, lane);
  if (w < 2) stage8<true>(x, rt * 64 + (w + 6) * 8, lds, (w + 6) * 8, lane);
  __syncthreads();
  if (!active) return;
  f32x4 acc[2][2];
#pragma unroll
  for (int m = 0; m < 2; ++m) { acc[m][0] = (f32x4){0.f, 0.f, 0.f, 0.f}; acc[m][1] = acc[m][0]; }
  wave_gemm<2>(bw, lds, mh * 2, lane, acc);
  const int g = lane >> 4;
#pragma unroll
  for (int m = 0; m < 2; ++m) {
    const int row = rt * 64 + (mh * 2 + m) * 16 + (lane & 15);
    if (panel < 18) {
      const float c = 0.23570226039551584f * 1.4426950408889634f;
      u32x4 v;
      v[0] = pk2(acc[m][0][0] * c, acc[m][0][1] * c); v[1] = pk2(acc[m][0][2] * c, acc[m][0][3] * c);
      v[2] = pk2(acc[m][1][0] * c, acc[m][1][1] * c); v[3] = pk2(acc[m][1][2] * c, acc[m][1][3] * c);
      *(u32x4*)(q + (size_t)row * D_ + panel * 32 + g * 8) = v;
    } else if (panel == 18) {
      u32x4 v;
      v[0] = pk2(acc[m][0][0], acc[m][0][1]); v[1] = pk2(acc[m][0][2], acc[m][0][3]);
      v[2] = pk2(acc[m][1][0], acc[m][1][1]); v[3] = pk2(acc[m][1][2], acc[m][1][3]);
      if (g < 2) *(u32x4*)(kv + KV_KLO + row * 32 + g * 16) = v;
      else if (g == 2) *(unsigned*)(kv + KV_KHI + row * 4) = v[0];
    } else {
      const int T = row >> 5, kk = row & 31;
      const int s = kk >> 4, h = (kk >> 2) & 1, j = ((kk >> 3) & 1) * 4 + (kk & 3);
      u16* vb = (u16*)(kv + KV_VT + (((T * 2 + s) * 2 + h) * 19) * 16 + j * 2);
#pragma unroll
      for (int t = 0; t < 2; ++t)
#pragma unroll
        for (int r = 0; r < 4; ++r) {
          const int d = 8 * g + 4 * t + r;
          if (d < 18) vb[d * 8] = (u16)(pk2(acc[m][t][r], 0.f) & 0xffffu);
          else if (d == 18) vb[d * 8] = (u16)0x3F80;
        }
    }
  }
}

__global__ __launch_bounds__(384) void proj_res_kernel(const u16* __restrict__ o, const u32x4* __restrict__ wp,
                                                       const u16* __restrict__ xin, u16* __restrict__ xout) {
  extern __shared__ __attribute__((aligned(16))) char lds[];
  const int tid = threadIdx.x, lane = tid & 63, w = tid >> 6;
  const int b = blockIdx.x;
  const int mh = w / 3, pw = w % 3;
  const int rt = 4 * (b & 7) + (b >> 3) / 6, panel = 3 * ((b >> 3) % 6) + pw;
  bf16x8 bw[2][KS];
  STAMP(1, 0);
  load_panel(wp, panel, lane, bw);
  stage8<false>(o, rt * 64 + w * 8, lds, w * 8, lane);
  if (w < 2) stage8<false>(o, rt * 64 + (w + 6) * 8, lds, (w + 6) * 8, lane);
  const int g = lane >> 4;
  u32x4 xr[2];
#pragma unroll
  for (int m = 0; m < 2; ++m) {
    const int row = rt * 64 + (mh * 2 + m) * 16 + (lane & 15);
    xr[m] = *(const u32x4*)(xin + (size_t)row * D_ + panel * 32 + g * 8);
  }
  STAMP(1, 1);
  __syncthreads();
  STAMP(1, 2);
  f32x4 acc[2][2];
#pragma unroll
  for (int m = 0; m < 2; ++m) { acc[m][0] = (f32x4){0.f, 0.f, 0.f, 0.f}; acc[m][1] = acc[m][0]; }
  wave_gemm<2>(bw, lds, mh * 2, lane, acc);
  STAMP(1, 3);
#pragma unroll
  for (int m = 0; m < 2; ++m) {
    const int row = rt * 64 + (mh * 2 + m) * 16 + (lane & 15);
    u32x4 v;
#pragma unroll
    for (int j = 0; j < 4; ++j) {
      const float a0 = rbf(acc[m][j >> 1][(j & 1) * 2]), a1 = rbf(acc[m][j >> 1][(j & 1) * 2 + 1]);
      v[j] = pk2(bflo(xr[m][j]) + a0, bfhi(xr[m][j]) + a1);
    }
    *(u32x4*)(xout + (size_t)row * D_ + panel * 32 + g * 8) = v;
  }
  STAMP(1, 7);
}

constexpr int QLD = 232;
__global__ __launch_bounds__(448) void ln_qkv_local_kernel(const u16* __restrict__ x, const u32x4* __restrict__ wp,
                                                           u16* __restrict__ o) {
  extern __shared__ __attribute__((aligned(16))) char lds[];
  const int tid = threadIdx.x, lane = tid & 63, w = tid >> 6;
  const int b = blockIdx.x;
  const int rt = 8 * ((b & 7) >> 1) + (b >> 5), hg = 4 * (b & 1) + ((b >> 3) & 3);
  const int r0 = rt * 64 - 16;
  bf16x8 bw[2][KS];
  STAMP(2, 0);
  load_panel(wp, hg * 7 + w, lane, bw);
  stage8<true>(x, r0 + w * 8, lds, w * 8, lane);
  if (w < 3) stage8<true>(x, r0 + (w + 7) * 8, lds, (w + 7) * 8, lane);
  STAMP(2, 1);
  __syncthreads();
  STAMP(2, 2);
  f32x4 acc[5][2];
#pragma unroll
  for (int m = 0; m < 5; ++m) { acc[m][0] = (f32x4){0.f, 0.f, 0.f, 0.f}; acc[m][1] = acc[m][0]; }
  wave_gemm<5>(bw, lds, 0, lane, acc);
  STAMP(2, 3);
  __syncthreads();
  u16* t = (u16*)lds;
  const int g = lane >> 4;
#pragma unroll
  for (int m = 0; m < 5; ++m) {
    u32x4 v;
    v[0] = pk2(acc[m][0][0], acc[m][0][1]); v[1] = pk2(acc[m][0][2], acc[m][0][3]);
    v[2] = pk2(acc[m][1][0], acc[m][1][1]); v[3] = pk2(acc[m][1][2], acc[m][1][3]);
    *(u32x4*)(t + (m * 16 + (lane & 15)) * QLD + w * 32 + g * 8) = v;
  }
  __syncthreads();
  STAMP(2, 4);
  if (tid < 256) {
    const int hh = tid >> 6, rl = tid & 63;
    const int grow = rt * 64 + rl, lrow = rl + 16;
    const unsigned* qp = (const unsigned*)(t + lrow * QLD + hh * 18);
    float qf[18];
#pragma unroll
    for (int i = 0; i < 9; ++i) { unsigned u = qp[i]; qf[2 * i] = bflo(u); qf[2 * i + 1] = bfhi(u); }
    float lg[8];
    float mx = -3.0e38f;
#pragma unroll
    for (int wi = 0; wi < 8; ++wi) {
      const unsigned* kp = (const unsigned*)(t + (lrow - 2 * wi) * QLD + 72 + hh * 18);
      float s = 0.f;
#pragma unroll
      for (int i = 0; i < 9; ++i) { unsigned u = kp[i]; s += qf[2 * i] * bflo(u) + qf[2 * i + 1] * bfhi(u); }
      s *= 0.23570226039551584f;
      s = (grow - 2 * wi >= 0) ? s : -1e9f;
      lg[wi] = s; mx = fmaxf(mx, s);
    }
    float sum = 0.f;
#pragma unroll
    for (int wi = 0; wi < 8; ++wi) { lg[wi] = __expf(lg[wi] - mx); sum += lg[wi]; }
    const float inv = 1.f / sum;
    float of[18];
#pragma unroll
    for (int i = 0; i < 18; ++i) of[i] = 0.f;
#pragma unroll
    for (int wi = 0; wi < 8; ++wi) {
      const unsigned* vp = (const unsigned*)(t + (lrow - 2 * wi) * QLD + 144 + hh * 18);
      const float p = lg[wi] * inv;
#pragma unroll
      for (int i = 0; i < 9; ++i) { unsigned u = vp[i]; of[2 * i] += p * bflo(u); of[2 * i + 1] += p * bfhi(u); }
    }
    unsigned* op = (unsigned*)(o + (size_t)grow * D_ + (hg * 4 + hh) * 18);
#pragma unroll
    for (int i = 0; i < 9; ++i) op[i] = pk2(of[2 * i], of[2 * i + 1]);
  }
  STAMP(2, 7);
}

constexpr float ABIAS = 6.0f;
__device__ __forceinline__ float xhalf_max(float m) {
  auto rr = __builtin_amdgcn_permlane32_swap(__float_as_uint(m), __float_as_uint(m), false, false);
  return fmaxf(__uint_as_float(rr[0]), __uint_as_float(rr[1]));
}
__global__ __launch_bounds__(512, 2) void attn_kernel(const u16* __restrict__ q, const u32x4* __restrict__ kv,
                                                      u16* __restrict__ o) {
  extern __shared__ __attribute__((aligned(16))) char lds[];
  const int tid = threadIdx.x, lane = tid & 63, w = tid >> 6;
  const int r = lane & 31, h = lane >> 5;
  const int qt = blockIdx.x >> 2, head = (blockIdx.x & 3) * 8 + w;
  STAMP(0, 0);
  {
    u32x4 tmp[19];
#pragma unroll
    for (int i = 0; i < 19; ++i) { const int c = tid + i * 512; if (c < KV_BYTES / 16) tmp[i] = kv[c]; }
#pragma unroll
    for (int i = 0; i < 19; ++i) { const int c = tid + i * 512; if (c < KV_BYTES / 16) ((u32x4*)lds)[c] = tmp[i]; }
  }
  const int qrow = qt * 32 + r;
  const unsigned* qp = (const unsigned*)(q + (size_t)qrow * D_ + head * 18);
  u32x4 qlo_u, qhi_u = {0u, 0u, 0u, 0u};
#pragma unroll
  for (int i = 0; i < 4; ++i) qlo_u[i] = qp[h * 4 + i];
  if (h == 0) qhi_u[0] = qp[8];
  const bf16x8 qlo = __builtin_bit_cast(bf16x8, qlo_u), qhi = __builtin_bit_cast(bf16x8, qhi_u);
  STAMP(0, 1);
  __syncthreads();
  STAMP(0, 2);
  const char* kloP = lds + KV_KLO + r * 32 + h * 16;
  const char* khiP = lds + KV_KHI + r * 4;
  const int dcl = r < 19 ? r : 18;
  const char* vP = lds + KV_VT + h * 304 + dcl * 16;
  f32x16 negm, acc;
#pragma unroll
  for (int i = 0; i < 16; ++i) { negm[i] = 0.f; acc[i] = 0.f; }
  {
    bf16x8 klo = *(const bf16x8*)kloP;
    u32x4 kh = {*(const unsigned*)khiP, 0u, 0u, 0u};
    f32x16 s = MFMA32(klo, qlo, negm);
    s = MFMA32(__builtin_bit_cast(bf16x8, kh), qhi, s);
    float tm = s[0];
#pragma unroll
    for (int i = 1; i < 16; ++i) tm = fmaxf(tm, s[i]);
    tm = xhalf_max(tm) + ABIAS;
#pragma unroll
    for (int i = 0; i < 16; ++i) negm[i] = -tm;
  }
#define LOADK(KLO, KHI, T) { KLO = *(const bf16x8*)(kloP + (T) * 1024); KHI = *(const unsigned*)(khiP + (T) * 128); }
#define LOADV(V0, V1, T) { V0 = *(const bf16x8*)(vP + (T) * 1216); V1 = *(const bf16x8*)(vP + (T) * 1216 + 608); }
#define QK(SS, KLO, KHI) { u32x4 kh_ = {KHI, 0u, 0u, 0u}; SS = MFMA32(KLO, qlo, negm); SS = MFMA32(__builtin_bit_cast(bf16x8, kh_), qhi, SS); }
#define EXPP(SS) _Pragma("unroll") for (int i = 0; i < 4; ++i) { \
      p0[i] = pk2(__builtin_amdgcn_exp2f(SS[2 * i]), __builtin_amdgcn_exp2f(SS[2 * i + 1])); \
      p1[i] = pk2(__builtin_amdgcn_exp2f(SS[8 + 2 * i]), __builtin_amdgcn_exp2f(SS[8 + 2 * i + 1])); }
#define STEP(T, SC, SN, KLN, KHN, KL2, KH2, VC0, VC1, VN0, VN1) { \
    QK(SN, KLN, KHN); \
    LOADK(KL2, KH2, ((T) + 2) & 63); \
    LOADV(VN0, VN1, ((T) + 1) & 63); \
    u32x4 p0, p1; \
    EXPP(SC); \
    unsigned orr = p0[0] | p0[1] | p0[2] | p0[3] | p1[0] | p1[1] | p1[2] | p1[3]; \
    if (__builtin_expect(__any((orr & 0x40004000u) != 0u), 0)) { \
      float tm = SC[0]; \
      _Pragma("unroll") for (int i = 1; i < 16; ++i) tm = fmaxf(tm, SC[i]); \
      tm = xhalf_max(tm); \
      const float dl = fmaxf(tm + ABIAS, 0.f); \
      const float al = __builtin_amdgcn_exp2f(-dl); \
      _Pragma("unroll") for (int i = 0; i < 16; ++i) { acc[i] *= al; negm[i] -= dl; SC[i] -= dl; SN[i] -= dl; } \
      EXPP(SC); \
    } \
    acc = MFMA32(VC0, __builtin_bit_cast(bf16x8, p0), acc); \
    acc = MFMA32(VC1, __builtin_bit_cast(bf16x8, p1), acc); }
  f32x16 sA, sB;
  bf16x8 kA, kB, vA0, vA1, vB0, vB1;
  unsigned hA, hB;
  LOADK(kA, hA, 0);
  QK(sA, kA, hA);
  LOADK(kB, hB, 1);
  LOADV(vA0, vA1, 0);
  for (int t = 0; t < 64; t += 2) {
    STEP(t, sA, sB, kB, hB, kA, hA, vA0, vA1, vB0, vB1);
    STEP(t + 1, sB, sA, kA, hA, kB, hB, vB0, vB1, vA0, vA1);
  }
  STAMP(0, 3);
  auto rr = __builtin_amdgcn_permlane32_swap(__float_as_uint(acc[10]), __float_as_uint(acc[10]), false, false);
  const float l = __uint_as_float(rr[0]);
  const float inv = 1.f / l;
  unsigned* op = (unsigned*)(o + (size_t)qrow * D_ + head * 18);
  op[2 * h + 0] = pk2(acc[0] * inv, acc[1] * inv);
  op[2 * h + 1] = pk2(acc[2] * inv, acc[3] * inv);
  op[4 + 2 * h + 0] = pk2(acc[4] * inv, acc[5] * inv);
  op[4 + 2 * h + 1] = pk2(acc[6] * inv, acc[7] * inv);
  if (h == 0) op[8] = pk2(acc[8] * inv, acc[9] * inv);
  STAMP(0, 7);
}

extern "C" void kernel_launch(void* const* d_in, const int* in_sizes, int n_in, void* d_out, int out_size,
                              void* d_ws, size_t ws_size, hipStream_t stream) {
  const u16* x0 = (const u16*)d_in[0];
  const u16 *wq = (const u16*)d_in[1], *wk = (const u16*)d_in[2], *wv = (const u16*)d_in[3];
  const u16 *woa = (const u16*)d_in[4], *wqkv = (const u16*)d_in[6], *wob = (const u16*)d_in[7];
  char* ws = (char*)d_ws;
  u16* xs[5] = {(u16*)x0, (u16*)(ws + OFF_X1), (u16*)(ws + OFF_X2), (u16*)(ws + OFF_X3), (u16*)d_out};
  u16* qb = (u16*)(ws + OFF_Q);
  u16* ob = (u16*)(ws + OFF_O);
  char* kv = ws + OFF_KV;
  (void)hipFuncSetAttribute((const void*)attn_kernel, hipFuncAttributeMaxDynamicSharedMemorySize, KV_BYTES);
  (void)hipFuncSetAttribute((const void*)ln_qkv_a_kernel, hipFuncAttributeMaxDynamicSharedMemorySize, 64 * LDA);
  (void)hipFuncSetAttribute((const void*)proj_res_kernel, hipFuncAttributeMaxDynamicSharedMemorySize, 64 * LDA);
  (void)hipFuncSetAttribute((const void*)ln_qkv_local_kernel, hipFuncAttributeMaxDynamicSharedMemorySize, 80 * LDA);
  prep_kernel<<<112 * PANEL_CH / 256, 256, 0, stream>>>(wq, wk, wv, woa, wqkv, wob, (u32x4*)ws);
  for (int l = 0; l < 2; ++l) {
    ln_qkv_a_kernel<<<224, 384, 64 * LDA, stream>>>(xs[2 * l], (const u32x4*)(ws + OFF_WQKVA), qb, kv);
    attn_kernel<<<256, 512, KV_BYTES, stream>>>(qb, (const u32x4*)kv, ob);
    proj_res_kernel<<<192, 384, 64 * LDA, stream>>>(ob, (const u32x4*)(ws + OFF_WOA), xs[2 * l], xs[2 * l + 1]);
    ln_qkv_local_kernel<<<256, 448, 80 * LDA, stream>>>(xs[2 * l + 1], (const u32x4*)(ws + OFF_WQKVB), ob);
    proj_res_kernel<<<192, 384, 64 * LDA, stream>>>(ob, (const u32x4*)(ws + OFF_WOB), xs[2 * l + 1], xs[2 * l + 2]);
  }
}
```

```cpp
#include <hip/hip_runtime.h>
#include <hip/hip_bf16.h>
#include <stdint.h>

typedef __attribute__((ext_vector_type(8))) short bf16x8;
typedef __attribute__((ext_vector_type(4))) float f32x4;
typedef __attribute__((ext_vector_type(2))) float f32x2;
typedef __attribute__((ext_vector_type(16))) float f32x16;
typedef __attribute__((ext_vector_type(4))) unsigned u32x4;
typedef __attribute__((ext_vector_type(2))) __bf16 bf16x2_t;
typedef unsigned short u16;

constexpr int S_ = 2048, D_ = 576;
constexpr int KS = 18;
constexpr int LDA = 1168;
constexpr int PANEL_CH = 2 * KS * 64;

constexpr size_t PANEL_B = (size_t)PANEL_CH * 16;
constexpr size_t OFF_WQKVA = 0;
constexpr size_t OFF_WOA = OFF_WQKVA + 20 * PANEL_B;
constexpr size_t OFF_WQKVB = OFF_WOA + 18 * PANEL_B;
constexpr size_t OFF_WOB = OFF_WQKVB + 56 * PANEL_B;
constexpr size_t ACT_B = (size_t)S_ * D_ * 2;
constexpr size_t OFF_Q = OFF_WOB + 18 * PANEL_B;
constexpr size_t OFF_O = OFF_Q + ACT_B;
constexpr size_t OFF_X1 = OFF_O + ACT_B;
constexpr size_t OFF_X2 = OFF_X1 + ACT_B;
constexpr size_t OFF_X3 = OFF_X2 + ACT_B;
constexpr size_t OFF_KV = OFF_X3 + ACT_B;
constexpr size_t OFF_CS = OFF_KV + 262144;
constexpr int KV_REC = 2368, KV_KHI = 1024, KV_VT = 1152;
constexpr int KV_BYTES = 64 * KV_REC;
constexpr int ATT_LDS = 66 * KV_REC;

#define STAMP(K, I)
__device__ __forceinline__ unsigned pk2(float a, float b) {
  f32x2 v = {a, b};
  bf16x2_t r = __builtin_convertvector(v, bf16x2_t);
  return __builtin_bit_cast(unsigned, r);
}
__device__ __forceinline__ float bflo(unsigned u) { return __uint_as_float(u << 16); }
__device__ __forceinline__ float bfhi(unsigned u) { return __uint_as_float(u & 0xffff0000u); }
__device__ __forceinline__ float rbf(float a) { return bflo(pk2(a, 0.f) & 0xffffu); }

__global__ __launch_bounds__(576) void prep_kernel(const u16* __restrict__ wq, const u16* __restrict__ wk,
                                                   const u16* __restrict__ wv, const u16* __restrict__ woa,
                                                   const u16* __restrict__ wqkv, const u16* __restrict__ wob,
                                                   u32x4* __restrict__ out, float* __restrict__ cs) {
  __shared__ __attribute__((aligned(16))) u16 tile[576 * 40];
  __shared__ float red[9][32];
  const int panel = blockIdx.x, tid = threadIdx.x, lane = tid & 63, w = tid >> 6;
  if (panel == 18 || panel == 19) {
    const u16* src = panel == 18 ? wk : wv;
#pragma unroll
    for (int c = 0; c < 32; ++c) tile[tid * 40 + c] = c < 18 ? src[tid * 18 + c] : (u16)0;
  } else {
    const int c8 = (tid & 3) * 8;
    const u16* src; int ld, col; bool ok = true;
    if (panel < 18) { src = wq; ld = 576; col = 32 * panel + c8; }
    else if (panel < 38) { src = woa; ld = 576; col = 32 * (panel - 20) + c8; }
    else if (panel < 94) {
      const int p = panel - 38, g = p / 7, cc = 32 * (p % 7) + c8;
      src = wqkv; ld = 1728; ok = cc < 216; col = ok ? (cc / 72) * 576 + g * 72 + (cc % 72) : 0;
    } else { src = wob; ld = 576; col = 32 * (panel - 94) + c8; }
    u32x4 v[4];
#pragma unroll
    for (int i = 0; i < 4; ++i) v[i] = *(const u32x4*)(src + (size_t)((tid >> 2) + 144 * i) * ld + col);
#pragma unroll
    for (int i = 0; i < 4; ++i) {
      if (!ok) v[i] = (u32x4){0u, 0u, 0u, 0u};
      *(u32x4*)(tile + ((tid >> 2) + 144 * i) * 40 + c8) = v[i];
    }
  }
  __syncthreads();
  const int i = lane & 15;
#pragma unroll
  for (int t = 0; t < 2; ++t) {
    const int c = 8 * (i >> 2) + 4 * t + (i & 3);
    float sum = 0.f;
#pragma unroll
    for (int h = 0; h < 2; ++h) {
      const int ks = w + 9 * h;
      const u16* tp = tile + (32 * ks + 8 * (lane >> 4)) * 40 + c;
      u32x4 v;
#pragma unroll
      for (int j = 0; j < 4; ++j) {
        const unsigned lo = tp[(2 * j) * 40], hi = tp[(2 * j + 1) * 40];
        v[j] = lo | (hi << 16);
        sum += bflo(v[j]) + bfhi(v[j]);
      }
      out[(size_t)panel * PANEL_CH + (t * KS + ks) * 64 + lane] = v;
    }
    sum += __shfl_xor(sum, 16); sum += __shfl_xor(sum, 32);
    if (lane < 16) red[w][c] = sum;
  }
  __syncthreads();
  if (tid < 32) {
    float sum = 0.f;
#pragma unroll
    for (int k = 0; k < 9; ++k) sum += red[k][tid];
    cs[panel * 32 + tid] = sum;
  }
}

__device__ __forceinline__ void load8(const u16* __restrict__ src, int grow0, int lane, u32x4 (&v)[9]) {
  int grow = grow0 + (lane >> 3); grow = grow < 0 ? 0 : grow;
  const u32x4* p = (const u32x4*)(src + (size_t)grow * D_) + (lane & 7);
#pragma unroll
  for (int i = 0; i < 9; ++i) v[i] = p[8 * i];
}
__device__ __forceinline__ float dot2(unsigned a, unsigned b, float c) {
  return __builtin_amdgcn_fdot2_f32_bf16(__builtin_bit_cast(bf16x2_t, a), __builtin_bit_cast(bf16x2_t, b), c, false);
}
template <bool LN>
__device__ __forceinline__ void put8(u32x4 (&v)[9], char* lds, f32x2* st, int lr0, int lane) {
  char* d = lds + (lr0 + (lane >> 3)) * LDA + (lane & 7) * 16;
#pragma unroll
  for (int i = 0; i < 9; ++i) *(u32x4*)(d + 128 * i) = v[i];
  if (LN) {
    float s0 = 0.f, s1 = 0.f, q0 = 0.f, q1 = 0.f;
#pragma unroll
    for (int i = 0; i < 9; ++i)
#pragma unroll
      for (int j = 0; j < 4; j += 2) {
        s0 = dot2(v[i][j], 0x3F803F80u, s0); s1 = dot2(v[i][j + 1], 0x3F803F80u, s1);
        q0 = dot2(v[i][j], v[i][j], q0); q1 = dot2(v[i][j + 1], v[i][j + 1], q1);
      }
    float s = s0 + s1, q = q0 + q1;
    s += __shfl_xor(s, 1); q += __shfl_xor(q, 1);
    s += __shfl_xor(s, 2); q += __shfl_xor(q, 2);
    s += __shfl_xor(s, 4); q += __shfl_xor(q, 4);
    const float mean = s * (1.f / 576.f);
    const float var = fmaxf(q * (1.f / 576.f) - mean * mean, 0.f);
    if ((lane & 7) == 0) st[lr0 + (lane >> 3)] = (f32x2){mean, rsqrtf(var + 1e-5f)};
  }
}

#define MFMA16(a, b, c) __builtin_amdgcn_mfma_f32_16x16x32_bf16(a, b, c, 0, 0, 0)
#define MFMA32(a, b, c) __builtin_amdgcn_mfma_f32_32x32x16_bf16(a, b, c, 0, 0, 0)
#define SB __builtin_amdgcn_sched_barrier(0)
#define BAR0() asm volatile("s_barrier" ::: "memory")
#define BAR() asm volatile("s_waitcnt lgkmcnt(0)\n\ts_barrier" ::: "memory")

template <int PRE>
__device__ __forceinline__ void panel_head(const u32x4* __restrict__ p, bf16x8 (&b)[2][KS]) {
#pragma unroll
  for (int ks = 0; ks < PRE; ++ks) {
#pragma unroll
    for (int t = 0; t < 2; ++t) b[t][ks] = __builtin_bit_cast(bf16x8, p[(t * KS + ks) * 64]);
    SB;
  }
}
template <int MT, int PRE>
__device__ __forceinline__ void wave_gemm(const u32x4* __restrict__ p, bf16x8 (&b)[2][KS], const char* lds, int lane,
                                          f32x4 (&acc)[MT][2]) {
  const char* ap = lds + (lane & 15) * LDA + (lane >> 4) * 16;
  bf16x8 a[2][MT];
#pragma unroll
  for (int m = 0; m < MT; ++m) {
    acc[m][0] = (f32x4){0.f, 0.f, 0.f, 0.f}; acc[m][1] = acc[m][0];
    a[0][m] = *(const bf16x8*)(ap + m * 16 * LDA);
  }
#pragma unroll
  for (int ks = 0; ks < KS; ++ks) {
    if (ks + PRE < KS) {
#pragma unroll
      for (int t = 0; t < 2; ++t) b[t][ks + PRE] = __builtin_bit_cast(bf16x8, p[(t * KS + ks + PRE) * 64]);
    }
    if (ks + 1 < KS) {
#pragma unroll
      for (int m = 0; m < MT; ++m) a[(ks + 1) & 1][m] = *(const bf16x8*)(ap + m * 16 * LDA + (ks + 1) * 64);
    }
    SB;
#pragma unroll
    for (int m = 0; m < MT; ++m) {
      acc[m][0] = MFMA16(b[0][ks], a[ks & 1][m], acc[m][0]);
      acc[m][1] = MFMA16(b[1][ks], a[ks & 1][m], acc[m][1]);
    }
    SB;
  }
}
__device__ __forceinline__ void ln_fix(f32x4 (&acc)[2], const f32x4 (&cs)[2], f32x2 st, float scale) {
  const float rs = st[1] * scale;
#pragma unroll
  for (int t = 0; t < 2; ++t)
#pragma unroll
    for (int r = 0; r < 4; ++r) acc[t][r] = (acc[t][r] - st[0] * cs[t][r]) * rs;
}

constexpr int QA_LDS = 64 * LDA + 64 * 8;
__global__ __launch_bounds__(512) void ln_qkv_a_kernel(const u16* __restrict__ x, const u32x4* __restrict__ wp,
                                                       const float* __restrict__ csum, u16* __restrict__ q,
                                                       char* __restrict__ kv) {
  extern __shared__ __attribute__((aligned(16))) char lds[];
  const int tid = threadIdx.x, lane = tid & 63, w = tid >> 6;
  const int b = blockIdx.x;
  const int rt = 4 * (b & 7) + (b >> 3) / 7;
  const int panel = 3 * ((b >> 3) % 7) + w;
  f32x2* st = (f32x2*)(lds + 64 * LDA);
  u32x4 v8[9];
  load8(x, rt * 64 + w * 8, lane, v8);
  SB;
  BAR0();
  if (!(w < 3 && panel < 20)) {
    put8<true>(v8, lds, st, w * 8, lane);
    BAR();
    return;
  }
  const int g = lane >> 4;
  bf16x8 bw[2][KS];
  const u32x4* pp = wp + (size_t)panel * PANEL_CH + lane;
  panel_head<8>(pp, bw);
  put8<true>(v8, lds, st, w * 8, lane);
  BAR();
  f32x4 cs[2];
  cs[0] = *(const f32x4*)(csum + panel * 32 + g * 8); cs[1] = *(const f32x4*)(csum + panel * 32 + g * 8 + 4);
  f32x4 acc[4][2];
  wave_gemm<4, 8>(pp, bw, lds, lane, acc);
#pragma unroll
  for (int m = 0; m < 4; ++m) {
    const int row = rt * 64 + m * 16 + (lane & 15);
    ln_fix(acc[m], cs, st[m * 16 + (lane & 15)], panel < 18 ? 0.23570226039551584f * 1.4426950408889634f : 1.f);
    u32x4 v;
    v[0] = pk2(acc[m][0][0], acc[m][0][1]); v[1] = pk2(acc[m][0][2], acc[m][0][3]);
    v[2] = pk2(acc[m][1][0], acc[m][1][1]); v[3] = pk2(acc[m][1][2], acc[m][1][3]);
    if (panel < 18) {
      *(u32x4*)(q + (size_t)row * D_ + panel * 32 + g * 8) = v;
    } else if (panel == 18) {
      char* rec = kv + (row >> 5) * KV_REC;
      if (g < 2) *(u32x4*)(rec + (row & 31) * 32 + g * 16) = v;
      else if (g == 2) *(unsigned*)(rec + KV_KHI + (row & 31) * 4) = v[0];
    } else {
      const int T = row >> 5, kk = row & 31;
      const int s = kk >> 4, h = (kk >> 2) & 1, j = ((kk >> 3) & 1) * 4 + (kk & 3);
      u16* vb = (u16*)(kv + T * KV_REC + KV_VT + ((s * 2 + h) * 19) * 16 + j * 2);
#pragma unroll
      for (int e = 0; e < 8; ++e) {
        const int d = 8 * g + e;
        if (d < 18) vb[d * 8] = (u16)((v[e >> 1] >> ((e & 1) * 16)) & 0xffffu);
        else if (d == 18) vb[d * 8] = (u16)0x3F80;
      }
    }
  }
}

__global__ __launch_bounds__(512) void proj_res_kernel(const u16* __restrict__ o, const u32x4* __restrict__ wp,
                                                       const u16* __restrict__ xin, u16* __restrict__ xout) {
  extern __shared__ __attribute__((aligned(16))) char lds[];
  const int tid = threadIdx.x, lane = tid & 63, w = tid >> 6;
  const int b = blockIdx.x;
  const int rt = 4 * (b & 7) + (b >> 3) / 6, panel = 3 * ((b >> 3) % 6) + w;
  const int g = lane >> 4;
  STAMP(1, 0);
  u32x4 v8[9];
  load8(o, rt * 64 + w * 8, lane, v8);
  SB;
  BAR0();
  if (w >= 3) {
    put8<false>(v8, lds, nullptr, w * 8, lane);
    BAR();
    return;
  }
  bf16x8 bw[2][KS];
  u32x4 xr[4];
  const u32x4* pp = wp + (size_t)panel * PANEL_CH + lane;
  panel_head<8>(pp, bw);
  put8<false>(v8, lds, nullptr, w * 8, lane);
  STAMP(1, 1);
  BAR();
  STAMP(1, 2);
#pragma unroll
  for (int m = 0; m < 4; ++m) {
    const int row = rt * 64 + m * 16 + (lane & 15);
    xr[m] = *(const u32x4*)(xin + (size_t)row * D_ + panel * 32 + g * 8);
  }
  f32x4 acc[4][2];
  wave_gemm<4, 8>(pp, bw, lds, lane, acc);
  STAMP(1, 3);
#pragma unroll
  for (int m = 0; m < 4; ++m) {
    const int row = rt * 64 + m * 16 + (lane & 15);
    u32x4 v;
#pragma unroll
    for (int j = 0; j < 4; ++j)
      v[j] = pk2(bflo(xr[m][j]) + acc[m][j >> 1][(j & 1) * 2], bfhi(xr[m][j]) + acc[m][j >> 1][(j & 1) * 2 + 1]);
    *(u32x4*)(xout + (size_t)row * D_ + panel * 32 + g * 8) = v;
  }
  STAMP(1, 7);
}

constexpr int QLD = 109;
constexpr int QOFF = 80 * LDA;
constexpr int STOFF = QOFF + 80 * QLD * 4;
constexpr int LOC_LDS = STOFF + 80 * 8;
__global__ __launch_bounds__(512) void ln_qkv_local_kernel(const u16* __restrict__ x, const u32x4* __restrict__ wp,
                                                           const float* __restrict__ csum, u16* __restrict__ o) {
  extern __shared__ __attribute__((aligned(16))) char lds[];
  const int tid = threadIdx.x, lane = tid & 63, w = tid >> 6;
  const int b = blockIdx.x;
  const int rt = 8 * ((b & 7) >> 1) + (b >> 5), hg = 4 * (b & 1) + ((b >> 3) & 3);
  const int r0 = rt * 64 - 16;
  unsigned* tq = (unsigned*)(lds + QOFF);
  f32x2* st = (f32x2*)(lds + STOFF);
  STAMP(2, 0);
  if (w < 7) {
    const int g = lane >> 4;
    u32x4 v8[9];
    load8(x, r0 + w * 8, lane, v8);
    SB;
    BAR0();
    bf16x8 bw[2][KS];
    const u32x4* pp = wp + (size_t)(hg * 7 + w) * PANEL_CH + lane;
    panel_head<4>(pp, bw);
    put8<true>(v8, lds, st, w * 8, lane);
    STAMP(2, 1);
    BAR();
    STAMP(2, 2);
    f32x4 cs[2];
    cs[0] = *(const f32x4*)(csum + (hg * 7 + w) * 32 + g * 8); cs[1] = *(const f32x4*)(csum + (hg * 7 + w) * 32 + g * 8 + 4);
    f32x4 acc[5][2];
    wave_gemm<5, 4>(pp, bw, lds, lane, acc);
    STAMP(2, 3);
    if (w * 4 + g < 27) {
#pragma unroll
      for (int m = 0; m < 5; ++m) {
        ln_fix(acc[m], cs, st[m * 16 + (lane & 15)], 1.f);
        unsigned* d = tq + (m * 16 + (lane & 15)) * QLD + w * 16 + g * 4;
        d[0] = pk2(acc[m][0][0], acc[m][0][1]); d[1] = pk2(acc[m][0][2], acc[m][0][3]);
        d[2] = pk2(acc[m][1][0], acc[m][1][1]); d[3] = pk2(acc[m][1][2], acc[m][1][3]);
      }
    }
  } else {
    u32x4 va[9], vb[9], vc[9];
    load8(x, r0 + 56, lane, va);
    load8(x, r0 + 64, lane, vb);
    load8(x, r0 + 72, lane, vc);
    SB;
    BAR0();
    put8<true>(va, lds, st, 56, lane);
    put8<true>(vb, lds, st, 64, lane);
    put8<true>(vc, lds, st, 72, lane);
    BAR();
  }
  BAR();
  STAMP(2, 4);
  {
    const int hh = w >> 1, rl = (w & 1) * 32 + (lane & 31), sub = lane >> 5;
    const int grow = rt * 64 + rl, lrow = rl + 16;
    const unsigned* qp = tq + lrow * QLD + hh * 9;
    unsigned qv[9];
#pragma unroll
    for (int i = 0; i < 9; ++i) qv[i] = qp[i];
    float lg[8];
#pragma unroll
    for (int wi = 0; wi < 4; ++wi) {
      const int ww = sub * 4 + wi;
      const unsigned* kp = qp + 36 - 2 * ww * QLD;
      float s0 = 0.f, s1 = 0.f;
#pragma unroll
      for (int i = 0; i < 8; i += 2) { s0 = dot2(qv[i], kp[i], s0); s1 = dot2(qv[i + 1], kp[i + 1], s1); }
      s0 = dot2(qv[8], kp[8], s0);
      float s = (s0 + s1) * 0.23570226039551584f;
      s = (grow - 2 * ww >= 0) ? s : -1e9f;
      auto rr = __builtin_amdgcn_permlane32_swap(__float_as_uint(s), __float_as_uint(s), false, false);
      lg[wi] = __uint_as_float(rr[0]); lg[4 + wi] = __uint_as_float(rr[1]);
    }
    float mx = lg[0];
#pragma unroll
    for (int wi = 1; wi < 8; ++wi) mx = fmaxf(mx, lg[wi]);
    float sum = 0.f;
#pragma unroll
    for (int wi = 0; wi < 8; ++wi) { lg[wi] = __expf(lg[wi] - mx); sum += lg[wi]; }
    const float inv = 1.f / sum;
    float of[10];
#pragma unroll
    for (int i = 0; i < 10; ++i) of[i] = 0.f;
    const unsigned* vp0 = qp + 72 + sub * 4;
#pragma unroll
    for (int wi = 0; wi < 8; ++wi) {
      const unsigned* vp = vp0 - 2 * wi * QLD;
      const float p = lg[wi] * inv;
#pragma unroll
      for (int i = 0; i < 5; ++i) { unsigned u = vp[i]; of[2 * i] += p * bflo(u); of[2 * i + 1] += p * bfhi(u); }
    }
    unsigned* op = (unsigned*)(o + (size_t)grow * D_ + (hg * 4 + hh) * 18) + sub * 4;
    if (sub == 0) op[0] = pk2(of[0], of[1]);
#pragma unroll
    for (int i = 1; i < 5; ++i) op[i] = pk2(of[2 * i], of[2 * i + 1]);
  }
  STAMP(2, 7);
}

__device__ __forceinline__ float xhalf_max(float m) {
  auto rr = __builtin_amdgcn_permlane32_swap(__float_as_uint(m), __float_as_uint(m), false, false);
  return fmaxf(__uint_as_float(rr[0]), __uint_as_float(rr[1]));
}
#define EX2(x) __builtin_amdgcn_exp2f(x)
__global__ __launch_bounds__(512, 2) void attn_kernel(const u16* __restrict__ q, const u32x4* __restrict__ kv,
                                                      u16* __restrict__ o) {
  extern __shared__ __attribute__((aligned(16))) char lds[];
  const int tid = threadIdx.x, lane = tid & 63, w = tid >> 6;
  const int r = lane & 31, h = lane >> 5;
  const int qt = blockIdx.x >> 2, head = (blockIdx.x & 3) * 8 + w;
  STAMP(0, 0);
  constexpr int CH = 16 * KV_REC / 16;
  const int c4 = tid + 2048 < CH ? tid + 2048 : CH - 1;
  u32x4 t0[5], t1[5], t2[5], t3[5];
#pragma unroll
  for (int i = 0; i < 4; ++i) t0[i] = kv[tid + i * 512];
  t0[4] = kv[c4];
  const int qrow = qt * 32 + r;
  const unsigned* qp = (const unsigned*)(q + (size_t)qrow * D_ + head * 18);
  u32x4 qlo_u, qhi_u = {0u, 0u, 0u, 0u};
#pragma unroll
  for (int i = 0; i < 4; ++i) qlo_u[i] = qp[h * 4 + i];
  if (h == 0) qhi_u[0] = qp[8];
#pragma unroll
  for (int i = 0; i < 4; ++i) t1[i] = kv[CH + tid + i * 512];
  t1[4] = kv[CH + c4];
#pragma unroll
  for (int i = 0; i < 4; ++i) t2[i] = kv[2 * CH + tid + i * 512];
  t2[4] = kv[2 * CH + c4];
#pragma unroll
  for (int i = 0; i < 4; ++i) t3[i] = kv[3 * CH + tid + i * 512];
  t3[4] = kv[3 * CH + c4];
  u32x4* l4 = (u32x4*)lds;
#define PUTCH(TT, C) { _Pragma("unroll") for (int i = 0; i < 4; ++i) l4[(C) * CH + tid + i * 512] = TT[i]; l4[(C) * CH + c4] = TT[4]; }
  PUTCH(t0, 0);
  const bf16x8 qlo = __builtin_bit_cast(bf16x8, qlo_u), qhi = __builtin_bit_cast(bf16x8, qhi_u);
  STAMP(0, 1);
  BAR();
  STAMP(0, 2);
  const char* kloP = lds + r * 32 + h * 16;
  const char* khiP = lds + KV_KHI + r * 4;
  const int dcl = r < 19 ? r : 18;
  const char* vP = lds + KV_VT + h * 304 + dcl * 16;
  f32x16 negm, acc, sA, sB;
  bf16x8 kA, kB, vA0, vA1, vB0, vB1;
  u32x4 hA = {0u, 0u, 0u, 0u}, hB = {0u, 0u, 0u, 0u};
  u32x4 pA0, pA1, pB0 = {0u, 0u, 0u, 0u}, pB1 = {0u, 0u, 0u, 0u};
#pragma unroll
  for (int i = 0; i < 16; ++i) { negm[i] = 0.f; acc[i] = 0.f; }
#define BF(x) __builtin_bit_cast(bf16x8, x)
  kA = *(const bf16x8*)kloP; hA[0] = *(const unsigned*)khiP;
  kB = *(const bf16x8*)(kloP + KV_REC); hB[0] = *(const unsigned*)(khiP + KV_REC);
  {
    sA = MFMA32(kA, qlo, negm);
    sA = MFMA32(BF(hA), qhi, sA);
    float tm = sA[0];
#pragma unroll
    for (int i = 1; i < 16; ++i) tm = fmaxf(tm, sA[i]);
    tm = xhalf_max(tm);
#pragma unroll
    for (int i = 0; i < 16; ++i) { negm[i] = -tm; sA[i] -= tm; }
  }
  vB0 = BF(pB0); vB1 = BF(pB0);
#define E4(SS, PD, I0, J0) { PD[J0] = pk2(EX2(SS[I0]), EX2(SS[I0 + 1])); PD[J0 + 1] = pk2(EX2(SS[I0 + 2]), EX2(SS[I0 + 3])); }
#define STEP(OFF, SC, SN, KLN, KHN, KL2, KH2, VP0, VP1, VC0, VC1, PP0, PP1, PC0, PC1) { \
    SN = MFMA32(KLN, qlo, negm); \
    KL2 = *(const bf16x8*)(kloP + (OFF) + 2 * KV_REC); KH2[0] = *(const unsigned*)(khiP + (OFF) + 2 * KV_REC); \
    SB; E4(SC, PC0, 0, 0); SB; \
    acc = MFMA32(VP0, BF(PP0), acc); \
    VC0 = *(const bf16x8*)(vP + (OFF)); VC1 = *(const bf16x8*)(vP + (OFF) + 608); \
    SB; E4(SC, PC0, 4, 2); SB; \
    SN = MFMA32(BF(KHN), qhi, SN); \
    SB; E4(SC, PC1, 8, 0); SB; \
    acc = MFMA32(VP1, BF(PP1), acc); \
    SB; E4(SC, PC1, 12, 2); SB; }
  for (int it = 0; it < 32; ++it) {
    if ((it & 7) == 7 && it < 31) {
      if (it == 7) { PUTCH(t1, 1); } else if (it == 15) { PUTCH(t2, 2); } else { PUTCH(t3, 3); }
      BAR();
    }
    STEP(0, sA, sB, kB, hB, kA, hA, vB0, vB1, vA0, vA1, pB0, pB1, pA0, pA1);
    STEP(KV_REC, sB, sA, kA, hA, kB, hB, vA0, vA1, vB0, vB1, pA0, pA1, pB0, pB1);
    kloP += 2 * KV_REC; khiP += 2 * KV_REC; vP += 2 * KV_REC;
  }
  acc = MFMA32(vB0, BF(pB0), acc);
  acc = MFMA32(vB1, BF(pB1), acc);
  STAMP(0, 3);
  {
    float chk = 0.f;
#pragma unroll
    for (int i = 0; i < 11; ++i) chk += acc[i] * 0.f;
    if (__builtin_expect(__any(!(chk == 0.f)), 0)) {
      kloP = lds + r * 32 + h * 16; khiP = lds + KV_KHI + r * 4; vP = lds + KV_VT + h * 304 + dcl * 16;
      float m = -3.0e38f;
#pragma unroll
      for (int i = 0; i < 16; ++i) { acc[i] = 0.f; negm[i] = 0.f; }
      for (int t = 0; t < 64; ++t) {
        kA = *(const bf16x8*)(kloP + t * KV_REC); hA[0] = *(const unsigned*)(khiP + t * KV_REC);
        vA0 = *(const bf16x8*)(vP + t * KV_REC); vA1 = *(const bf16x8*)(vP + t * KV_REC + 608);
        sA = MFMA32(kA, qlo, negm);
        sA = MFMA32(BF(hA), qhi, sA);
        float tm = sA[0];
#pragma unroll
        for (int i = 1; i < 16; ++i) tm = fmaxf(tm, sA[i]);
        tm = fmaxf(xhalf_max(tm), m);
        const float al = EX2(m - tm);
        m = tm;
#pragma unroll
        for (int i = 0; i < 16; ++i) { acc[i] *= al; sA[i] -= tm; }
        E4(sA, pA0, 0, 0); E4(sA, pA0, 4, 2); E4(sA, pA1, 8, 0); E4(sA, pA1, 12, 2);
        acc = MFMA32(vA0, BF(pA0), acc);
        acc = MFMA32(vA1, BF(pA1), acc);
      }
    }
  }
  auto rr = __builtin_amdgcn_permlane32_swap(__float_as_uint(acc[10]), __float_as_uint(acc[10]), false, false);
  const float l = __uint_as_float(rr[0]);
  const float inv = 1.f / l;
  unsigned* op = (unsigned*)(o + (size_t)qrow * D_ + head * 18);
  op[2 * h + 0] = pk2(acc[0] * inv, acc[1] * inv);
  op[2 * h + 1] = pk2(acc[2] * inv, acc[3] * inv);
  op[4 + 2 * h + 0] = pk2(acc[4] * inv, acc[5] * inv);
  op[4 + 2 * h + 1] = pk2(acc[6] * inv, acc[7] * inv);
  if (h == 0) op[8] = pk2(acc[8] * inv, acc[9] * inv);
  STAMP(0, 7);
}

extern "C" void kernel_launch(void* const* d_in, const int* in_sizes, int n_in, void* d_out, int out_size,
                              void* d_ws, size_t ws_size, hipStream_t stream) {
  const u16* x0 = (const u16*)d_in[0];
  const u16 *wq = (const u16*)d_in[1], *wk = (const u16*)d_in[2], *wv = (const u16*)d_in[3];
  const u16 *woa = (const u16*)d_in[4], *wqkv = (const u16*)d_in[6], *wob = (const u16*)d_in[7];
  char* ws = (char*)d_ws;
  u16* xs[5] = {(u16*)x0, (u16*)(ws + OFF_X1), (u16*)(ws + OFF_X2), (u16*)(ws + OFF_X3), (u16*)d_out};
  u16* qb = (u16*)(ws + OFF_Q);
  u16* ob = (u16*)(ws + OFF_O);
  char* kv = ws + OFF_KV;
  const float* cs = (const float*)(ws + OFF_CS);
  (void)hipFuncSetAttribute((const void*)attn_kernel, hipFuncAttributeMaxDynamicSharedMemorySize, ATT_LDS);
  (void)hipFuncSetAttribute((const void*)ln_qkv_a_kernel, hipFuncAttributeMaxDynamicSharedMemorySize, QA_LDS);
  (void)hipFuncSetAttribute((const void*)proj_res_kernel, hipFuncAttributeMaxDynamicSharedMemorySize, 64 * LDA);
  (void)hipFuncSetAttribute((const void*)ln_qkv_local_kernel, hipFuncAttributeMaxDynamicSharedMemorySize, LOC_LDS);
  prep_kernel<<<112, 576, 0, stream>>>(wq, wk, wv, woa, wqkv, wob, (u32x4*)ws, (float*)(ws + OFF_CS));
  for (int l = 0; l < 2; ++l) {
    ln_qkv_a_kernel<<<224, 512, QA_LDS, stream>>>(xs[2 * l], (const u32x4*)(ws + OFF_WQKVA), cs, qb, kv);
    attn_kernel<<<256, 512, ATT_LDS, stream>>>(qb, (const u32x4*)kv, ob);
    proj_res_kernel<<<192, 512, 64 * LDA, stream>>>(ob, (const u32x4*)(ws + OFF_WOA), xs[2 * l], xs[2 * l + 1]);
    ln_qkv_local_kernel<<<256, 512, LOC_LDS, stream>>>(xs[2 * l + 1], (const u32x4*)(ws + OFF_WQKVB), cs + 38 * 32, ob);
    proj_res_kernel<<<192, 512, 64 * LDA, stream>>>(ob, (const u32x4*)(ws + OFF_WOB), xs[2 * l + 1], xs[2 * l + 2]);
  }
}
```

```cpp
#include <hip/hip_runtime.h>
#include <hip/hip_bf16.h>
#include <stdint.h>

typedef __attribute__((ext_vector_type(8))) short bf16x8;
typedef __attribute__((ext_vector_type(4))) float f32x4;
typedef __attribute__((ext_vector_type(2))) float f32x2;
typedef __attribute__((ext_vector_type(16))) float f32x16;
typedef __attribute__((ext_vector_type(4))) unsigned u32x4;
typedef __attribute__((ext_vector_type(2))) __bf16 bf16x2_t;
typedef unsigned short u16;

constexpr int S_ = 2048, D_ = 576;
constexpr int KS = 18;
constexpr int LDA = 1168;
constexpr int PANEL_CH = 2 * KS * 64;

constexpr size_t PANEL_B = (size_t)PANEL_CH * 16;
constexpr size_t OFF_WQKVA = 0;
constexpr size_t OFF_WOA = OFF_WQKVA + 20 * PANEL_B;
constexpr size_t OFF_WQKVB = OFF_WOA + 18 * PANEL_B;
constexpr size_t OFF_WOB = OFF_WQKVB + 56 * PANEL_B;
constexpr size_t ACT_B = (size_t)S_ * D_ * 2;
constexpr size_t OFF_Q = OFF_WOB + 18 * PANEL_B;
constexpr size_t OFF_O = OFF_Q + ACT_B;
constexpr size_t OFF_X1 = OFF_O + ACT_B;
constexpr size_t OFF_X2 = OFF_X1 + ACT_B;
constexpr size_t OFF_X3 = OFF_X2 + ACT_B;
constexpr size_t OFF_KV = OFF_X3 + ACT_B;
constexpr size_t OFF_CS = OFF_KV + 262144;
constexpr int KV_REC = 2368, KV_KHI = 1024, KV_VT = 1152;
constexpr int KV_BYTES = 64 * KV_REC;
constexpr int ATT_LDS = 66 * KV_REC;

#define STAMP(K, I)
#define STW(K, I, T)
__device__ __forceinline__ unsigned pk2(float a, float b) {
  f32x2 v = {a, b};
  bf16x2_t r = __builtin_convertvector(v, bf16x2_t);
  return __builtin_bit_cast(unsigned, r);
}
__device__ __forceinline__ float bflo(unsigned u) { return __uint_as_float(u << 16); }
__device__ __forceinline__ float bfhi(unsigned u) { return __uint_as_float(u & 0xffff0000u); }
__device__ __forceinline__ float rbf(float a) { return bflo(pk2(a, 0.f) & 0xffffu); }

__global__ __launch_bounds__(576) void prep_kernel(const u16* __restrict__ wq, const u16* __restrict__ wk,
                                                   const u16* __restrict__ wv, const u16* __restrict__ woa,
                                                   const u16* __restrict__ wqkv, const u16* __restrict__ wob,
                                                   u32x4* __restrict__ out, float* __restrict__ cs) {
  __shared__ __attribute__((aligned(16))) u16 tile[576 * 40];
  __shared__ float red[9][32];
  const int panel = blockIdx.x, tid = threadIdx.x, lane = tid & 63, w = tid >> 6;
  STAMP(4, 0);
  if (panel == 18 || panel == 19) {
    const u16* src = panel == 18 ? wk : wv;
#pragma unroll
    for (int c = 0; c < 32; ++c) tile[tid * 40 + c] = c < 18 ? src[tid * 18 + c] : (u16)0;
  } else {
    const int c8 = (tid & 3) * 8;
    const u16* src; int ld, col; bool ok = true;
    if (panel < 18) { src = wq; ld = 576; col = 32 * panel + c8; }
    else if (panel < 38) { src = woa; ld = 576; col = 32 * (panel - 20) + c8; }
    else if (panel < 94) {
      const int p = panel - 38, g = p / 7, cc = 32 * (p % 7) + c8;
      src = wqkv; ld = 1728; ok = cc < 216; col = ok ? (cc / 72) * 576 + g * 72 + (cc % 72) : 0;
    } else { src = wob; ld = 576; col = 32 * (panel - 94) + c8; }
    u32x4 v[4];
#pragma unroll
    for (int i = 0; i < 4; ++i) v[i] = *(const u32x4*)(src + (size_t)((tid >> 2) + 144 * i) * ld + col);
#pragma unroll
    for (int i = 0; i < 4; ++i) {
      if (!ok) v[i] = (u32x4){0x3F80u, 0u, 0x3F80u, 0u};
      *(u32x4*)(tile + ((tid >> 2) + 144 * i) * 40 + c8) = v[i];
    }
  }
  STAMP(4, 1);
  __syncthreads();
  STAMP(4, 2);
  const int i = lane & 15;
#pragma unroll
  for (int t = 0; t < 2; ++t) {
    const int c = 8 * (i >> 2) + 4 * t + (i & 3);
    float sum = 0.f;
#pragma unroll
    for (int h = 0; h < 2; ++h) {
      const int ks = w + 9 * h;
      const u16* tp = tile + (32 * ks + 8 * (lane >> 4)) * 40 + c;
      u32x4 v;
#pragma unroll
      for (int j = 0; j < 4; ++j) {
        const unsigned lo = tp[(2 * j) * 40], hi = tp[(2 * j + 1) * 40];
        v[j] = lo | (hi << 16);
        sum += bflo(v[j]) + bfhi(v[j]);
      }
      out[(size_t)panel * PANEL_CH + (t * KS + ks) * 64 + lane] = v;
    }
    sum += __shfl_xor(sum, 16); sum += __shfl_xor(sum, 32);
    if (lane < 16) red[w][c] = sum;
  }
  STAMP(4, 3);
  __syncthreads();
  if (tid < 32) {
    float sum = 0.f;
#pragma unroll
    for (int k = 0; k < 9; ++k) sum += red[k][tid];
    cs[panel * 32 + tid] = sum;
  }
  STAMP(4, 7);
}

__device__ __forceinline__ void load8(const u16* __restrict__ src, int grow0, int lane, u32x4 (&v)[9]) {
  int grow = grow0 + (lane >> 3); grow = grow < 0 ? 0 : grow;
  const u32x4* p = (const u32x4*)(src + (size_t)grow * D_) + (lane & 7);
#pragma unroll
  for (int i = 0; i < 9; ++i) v[i] = p[8 * i];
}
__device__ __forceinline__ float dot2(unsigned a, unsigned b, float c) {
  return __builtin_amdgcn_fdot2_f32_bf16(__builtin_bit_cast(bf16x2_t, a), __builtin_bit_cast(bf16x2_t, b), c, false);
}
template <int C>
__device__ __forceinline__ float dppf(float v) {
  return __int_as_float(__builtin_amdgcn_update_dpp(0, __float_as_int(v), C, 0xf, 0xf, true));
}
__device__ __forceinline__ void stat_acc(const u32x4& v, f32x4& a) {
#pragma unroll
  for (int j = 0; j < 4; j += 2) {
    a[0] = dot2(v[j], 0x3F803F80u, a[0]); a[1] = dot2(v[j + 1], 0x3F803F80u, a[1]);
    a[2] = dot2(v[j], v[j], a[2]); a[3] = dot2(v[j + 1], v[j + 1], a[3]);
  }
}
__device__ __forceinline__ void stat_fin(const f32x4& a, f32x2* st, int lr0, int lane) {
  float s = a[0] + a[1], q = a[2] + a[3];
  s += dppf<0xB1>(s); q += dppf<0xB1>(q);
  s += dppf<0x4E>(s); q += dppf<0x4E>(q);
  s += dppf<0x141>(s); q += dppf<0x141>(q);
  const float mean = s * (1.f / 576.f);
  const float var = fmaxf(q * (1.f / 576.f) - mean * mean, 0.f);
  st[lr0 + (lane >> 3)] = (f32x2){mean, rsqrtf(var + 1e-5f)};
}
__device__ __forceinline__ void stat8(const u32x4 (&v)[9], f32x2* st, int lr0, int lane) {
  f32x4 a = {0.f, 0.f, 0.f, 0.f};
#pragma unroll
  for (int i = 0; i < 9; ++i) stat_acc(v[i], a);
  stat_fin(a, st, lr0, lane);
}
typedef __attribute__((address_space(3))) volatile unsigned lds_vu32;
__device__ __forceinline__ void flag_set(unsigned* fl, int w) {
  asm volatile("" ::: "memory");
  ((lds_vu32*)fl)[w] = 1u;
  asm volatile("" ::: "memory");
}
__device__ __forceinline__ void flag_wait(unsigned* fl) {
  while (true) {
    asm volatile("" ::: "memory");
    const unsigned a = ((lds_vu32*)fl)[0], b = ((lds_vu32*)fl)[1];
    if ((a & b) != 0u) break;
    __builtin_amdgcn_s_sleep(1);
  }
  asm volatile("" ::: "memory");
}
__device__ __forceinline__ f32x2 sq2st(f32x2 sq) {
  const float mean = sq[0] * (1.f / 576.f);
  const float var = fmaxf(sq[1] * (1.f / 576.f) - mean * mean, 0.f);
  return (f32x2){mean, rsqrtf(var + 1e-5f)};
}
template <bool LN>
__device__ __forceinline__ void put8(u32x4 (&v)[9], char* lds, f32x2* st, int lr0, int lane) {
  char* d = lds + (lr0 + (lane >> 3)) * LDA + (lane & 7) * 16;
#pragma unroll
  for (int i = 0; i < 9; ++i) *(u32x4*)(d + 128 * i) = v[i];
  if (LN) stat8(v, st, lr0, lane);
}

#define MFMA16(a, b, c) __builtin_amdgcn_mfma_f32_16x16x32_bf16(a, b, c, 0, 0, 0)
#define MFMA32(a, b, c) __builtin_amdgcn_mfma_f32_32x32x16_bf16(a, b, c, 0, 0, 0)
#define SB __builtin_amdgcn_sched_barrier(0)
#define BF(x) __builtin_bit_cast(bf16x8, x)
#define BAR0() asm volatile("s_barrier" ::: "memory")
#define BAR() asm volatile("s_waitcnt lgkmcnt(0)\n\ts_barrier" ::: "memory")

template <int PRE, int NT = 2>
__device__ __forceinline__ void panel_head(const u32x4* __restrict__ p, bf16x8 (&b)[NT][KS]) {
#pragma unroll
  for (int ks = 0; ks < PRE; ++ks) {
#pragma unroll
    for (int t = 0; t < NT; ++t) b[t][ks] = __builtin_bit_cast(bf16x8, p[(t * KS + ks) * 64]);
    SB;
  }
}
template <int MT, int PRE, int NT = 2, int NG = 0, int G0 = 0>
__device__ __forceinline__ void wave_gemm(const u32x4* __restrict__ p, bf16x8 (&b)[NT][KS], const char* lds, int lane,
                                          f32x4 (&acc)[MT][NT], f32x4* gr = nullptr) {
  const char* ap = lds + (lane & 15) * LDA + (lane >> 4) * 16;
  bf16x8 a[2][MT];
#pragma unroll
  for (int n = 0; n < NG; ++n) gr[n] = (f32x4){0.f, 0.f, 0.f, 0.f};
#pragma unroll
  for (int m = 0; m < MT; ++m) {
#pragma unroll
    for (int t = 0; t < NT; ++t) acc[m][t] = (f32x4){0.f, 0.f, 0.f, 0.f};
    a[0][m] = *(const bf16x8*)(ap + m * 16 * LDA);
  }
#pragma unroll
  for (int ks = 0; ks < KS; ++ks) {
    if (ks + PRE < KS) {
#pragma unroll
      for (int t = 0; t < NT; ++t) b[t][ks + PRE] = __builtin_bit_cast(bf16x8, p[(t * KS + ks + PRE) * 64]);
    }
    if (ks + 1 < KS) {
#pragma unroll
      for (int m = 0; m < MT; ++m) a[(ks + 1) & 1][m] = *(const bf16x8*)(ap + m * 16 * LDA + (ks + 1) * 64);
    }
    SB;
#pragma unroll
    for (int m = 0; m < MT; ++m)
#pragma unroll
      for (int t = 0; t < NT; ++t) acc[m][t] = MFMA16(b[t][ks], a[ks & 1][m], acc[m][t]);
#pragma unroll
    for (int n = 0; n < NG; ++n) gr[n] = MFMA16(a[ks & 1][G0 + n], a[ks & 1][G0 + n], gr[n]);
    SB;
  }
#pragma unroll
  for (int m = 0; m < MT; ++m)
#pragma unroll
    for (int t = 0; t < NT; ++t) asm volatile("" : "+v"(acc[m][t]));
#pragma unroll
  for (int n = 0; n < NG; ++n) asm volatile("" : "+v"(gr[n]));
}
__device__ __forceinline__ void ln_fix(f32x4 (&acc)[2], const f32x4 (&cs)[2], f32x2 st, float scale) {
  const float rs = st[1] * scale;
#pragma unroll
  for (int t = 0; t < 2; ++t)
#pragma unroll
    for (int r = 0; r < 4; ++r) acc[t][r] = (acc[t][r] - st[0] * cs[t][r]) * rs;
}

constexpr int QA_LDS = 64 * LDA + 64 * 8;
__global__ __launch_bounds__(512) void ln_qkv_a_kernel(const u16* __restrict__ x, const u32x4* __restrict__ wp,
                                                       const float* __restrict__ csum, u16* __restrict__ q,
                                                       char* __restrict__ kv) {
  extern __shared__ __attribute__((aligned(16))) char lds[];
  const int tid = threadIdx.x, lane = tid & 63, w = tid >> 6;
  const int b = blockIdx.x;
  const int rt = 4 * (b & 7) + (b >> 3) / 7;
  const int panel = 3 * ((b >> 3) % 7) + w;
  f32x2* st = (f32x2*)(lds + 64 * LDA);
  u32x4 v8[9];
  load8(x, rt * 64 + w * 8, lane, v8);
  SB;
  BAR0();
  if (!(w < 3 && panel < 20)) {
    put8<true>(v8, lds, st, w * 8, lane);
    BAR();
    return;
  }
  const int g = lane >> 4;
  bf16x8 bw[2][KS];
  const u32x4* pp = wp + (size_t)panel * PANEL_CH + lane;
  panel_head<8>(pp, bw);
  put8<true>(v8, lds, st, w * 8, lane);
  BAR();
  f32x4 cs[2];
  cs[0] = *(const f32x4*)(csum + panel * 32 + g * 8); cs[1] = *(const f32x4*)(csum + panel * 32 + g * 8 + 4);
  f32x4 acc[4][2];
  wave_gemm<4, 8>(pp, bw, lds, lane, acc);
#pragma unroll
  for (int m = 0; m < 4; ++m) {
    const int row = rt * 64 + m * 16 + (lane & 15);
    ln_fix(acc[m], cs, st[m * 16 + (lane & 15)], panel < 18 ? 0.23570226039551584f * 1.4426950408889634f : 1.f);
    u32x4 v;
    v[0] = pk2(acc[m][0][0], acc[m][0][1]); v[1] = pk2(acc[m][0][2], acc[m][0][3]);
    v[2] = pk2(acc[m][1][0], acc[m][1][1]); v[3] = pk2(acc[m][1][2], acc[m][1][3]);
    if (panel < 18) {
      *(u32x4*)(q + (size_t)row * D_ + panel * 32 + g * 8) = v;
    } else if (panel == 18) {
      char* rec = kv + (row >> 5) * KV_REC;
      if (g < 2) *(u32x4*)(rec + (row & 31) * 32 + g * 16) = v;
      else if (g == 2) *(unsigned*)(rec + KV_KHI + (row & 31) * 4) = v[0];
    } else {
      const int T = row >> 5, kk = row & 31;
      const int s = kk >> 4, h = (kk >> 2) & 1, j = ((kk >> 3) & 1) * 4 + (kk & 3);
      u16* vb = (u16*)(kv + T * KV_REC + KV_VT + ((s * 2 + h) * 19) * 16 + j * 2);
#pragma unroll
      for (int e = 0; e < 8; ++e) {
        const int d = 8 * g + e;
        if (d < 18) vb[d * 8] = (u16)((v[e >> 1] >> ((e & 1) * 16)) & 0xffffu);
        else if (d == 18) vb[d * 8] = (u16)0x3F80;
      }
    }
  }
}

__global__ __launch_bounds__(512) void proj_res_kernel(const u16* __restrict__ o, const u32x4* __restrict__ wp,
                                                       const u16* __restrict__ xin, u16* __restrict__ xout) {
  extern __shared__ __attribute__((aligned(16))) char lds[];
  const int tid = threadIdx.x, lane = tid & 63, w = tid >> 6;
  const int b = blockIdx.x;
  const int rt = 4 * (b & 7) + (b >> 3) / 6, panel = 3 * ((b >> 3) % 6) + w;
  const int g = lane >> 4;
  STAMP(1, 0);
  u32x4 v8[9];
  load8(o, rt * 64 + w * 8, lane, v8);
  SB;
  BAR0();
  if (w >= 3) {
    put8<false>(v8, lds, nullptr, w * 8, lane);
    BAR();
    return;
  }
  bf16x8 bw[2][KS];
  u32x4 xr[4];
  const u32x4* pp = wp + (size_t)panel * PANEL_CH + lane;
  panel_head<8>(pp, bw);
  put8<false>(v8, lds, nullptr, w * 8, lane);
  STAMP(1, 1);
  BAR();
  STAMP(1, 2);
#pragma unroll
  for (int m = 0; m < 4; ++m) {
    const int row = rt * 64 + m * 16 + (lane & 15);
    xr[m] = *(const u32x4*)(xin + (size_t)row * D_ + panel * 32 + g * 8);
  }
  f32x4 acc[4][2];
  wave_gemm<4, 8>(pp, bw, lds, lane, acc);
  STAMP(1, 3);
#pragma unroll
  for (int m = 0; m < 4; ++m) {
    const int row = rt * 64 + m * 16 + (lane & 15);
    u32x4 v;
#pragma unroll
    for (int j = 0; j < 4; ++j)
      v[j] = pk2(bflo(xr[m][j]) + acc[m][j >> 1][(j & 1) * 2], bfhi(xr[m][j]) + acc[m][j >> 1][(j & 1) * 2 + 1]);
    *(u32x4*)(xout + (size_t)row * D_ + panel * 32 + g * 8) = v;
  }
  STAMP(1, 7);
}

constexpr int QLD = 100, VLD = 88;
constexpr int QOFF = 80 * LDA;
constexpr int VOFF = QOFF + 80 * QLD * 4;
constexpr int STOFF = VOFF + 72 * VLD * 2;
constexpr int FLOFF = STOFF + 80 * 8;
constexpr int LOC_LDS = FLOFF + 32;
__device__ __forceinline__ int segoff(int dw) { return (dw / 9) * 12 + dw % 9; }
__device__ __forceinline__ void put_vt(u16* d, unsigned v) { d[0] = (u16)v; d[VLD] = (u16)(v >> 16); }
__device__ __forceinline__ float xrow_max(float m) {
  auto a = __builtin_amdgcn_permlane16_swap(__float_as_uint(m), __float_as_uint(m), false, false);
  m = fmaxf(__uint_as_float(a[0]), __uint_as_float(a[1]));
  auto b = __builtin_amdgcn_permlane32_swap(__float_as_uint(m), __float_as_uint(m), false, false);
  return fmaxf(__uint_as_float(b[0]), __uint_as_float(b[1]));
}
template <int MT>
__device__ __forceinline__ void loc_full(const u32x4* __restrict__ pp, bf16x8 (&bw)[2][KS], const float* __restrict__ csp,
                                         char* lds, int lane, int pl) {
  constexpr int M0 = 5 - MT;
  const int g = lane >> 4;
  f32x4 cs[2];
  cs[0] = *(const f32x4*)(csp + g * 8); cs[1] = *(const f32x4*)(csp + g * 8 + 4);
  f32x4 acc[MT][2];
  f32x2* st = (f32x2*)(lds + STOFF);
  unsigned* tq = (unsigned*)(lds + QOFF) + (M0 * 16 + (lane & 15)) * QLD;
  int so[4];
#pragma unroll
  for (int j = 0; j < 4; ++j) { so[j] = segoff(pl * 16 + g * 4 + j); asm volatile("" : "+v"(so[j])); }
  wave_gemm<MT, 4>(pp, bw, lds + M0 * 16 * LDA, lane, acc);
  STAMP(2, 3); STW(6, 3, 256);
  flag_wait((unsigned*)(lds + FLOFF));
  f32x2 sv[MT];
#pragma unroll
  for (int m = 0; m < MT; ++m) sv[m] = st[(M0 + m) * 16 + (lane & 15)];
  if (pl * 32 + g * 8 < 144) {
#pragma unroll
    for (int m = 0; m < MT; ++m) {
      ln_fix(acc[m], cs, sv[m], 1.f);
      unsigned* d = tq + m * 16 * QLD;
      d[so[0]] = pk2(acc[m][0][0], acc[m][0][1]); d[so[1]] = pk2(acc[m][0][2], acc[m][0][3]);
      d[so[2]] = pk2(acc[m][1][0], acc[m][1][1]); d[so[3]] = pk2(acc[m][1][2], acc[m][1][3]);
    }
  } else {
    u16* vt = (u16*)(lds + VOFF) + (pl * 32 + g * 8 - 144) * VLD + M0 * 16 + (lane & 15);
#pragma unroll
    for (int m = 0; m < MT; ++m) {
      ln_fix(acc[m], cs, sv[m], 1.f);
      put_vt(vt + m * 16, pk2(acc[m][0][0], acc[m][0][1])); put_vt(vt + m * 16 + 2 * VLD, pk2(acc[m][0][2], acc[m][0][3]));
      put_vt(vt + m * 16 + 4 * VLD, pk2(acc[m][1][0], acc[m][1][1])); put_vt(vt + m * 16 + 6 * VLD, pk2(acc[m][1][2], acc[m][1][3]));
    }
  }
}
__global__ __launch_bounds__(512) void ln_qkv_local_kernel(const u16* __restrict__ x, const u32x4* __restrict__ wp,
                                                           const float* __restrict__ csum, u16* __restrict__ o) {
  extern __shared__ __attribute__((aligned(16))) char lds[];
  const int tid = threadIdx.x, lane = tid & 63, w = tid >> 6;
  const int b = blockIdx.x;
  const int rt = 8 * ((b & 7) >> 1) + (b >> 5), hg = 4 * (b & 1) + ((b >> 3) & 3);
  const int r0 = rt * 64 - 16;
  unsigned* tq = (unsigned*)(lds + QOFF);
  f32x2* st = (f32x2*)(lds + STOFF);
  STAMP(2, 0); STW(5, 0, 384); STW(6, 0, 256);
  if (w < 6) {
    const int pl = w < 2 ? w : (w < 4 ? w + 2 : w - 2);
    u32x4 v8[1][9];
    load8(x, r0 + w * 8, lane, v8[0]);
    SB;
    BAR0();
    bf16x8 bw[2][KS];
    const u32x4* pp = wp + (size_t)(hg * 7 + pl) * PANEL_CH + lane;
    panel_head<4>(pp, bw);
    put8<false>(v8[0], lds, st, w * 8, lane);
    if (tid < 8) ((unsigned*)(lds + FLOFF))[tid] = 0u;
    STAMP(2, 1);
    BAR();
    STAMP(2, 2);
    if (w < 2) loc_full<4>(pp, bw, csum + (hg * 7 + pl) * 32, lds, lane, pl);
    else loc_full<5>(pp, bw, csum + (hg * 7 + pl) * 32, lds, lane, pl);
  } else {
    const int t = w - 6, g = lane >> 4;
    u32x4 v8[2][9];
    load8(x, r0 + w * 8, lane, v8[0]);
    load8(x, r0 + w * 8 + 16, lane, v8[1]);
    SB;
    BAR0();
    bf16x8 bw[1][KS];
    const u32x4* pp = wp + (size_t)(hg * 7 + 6) * PANEL_CH + t * KS * 64 + lane;
    put8<false>(v8[0], lds, st, w * 8, lane);
    put8<false>(v8[1], lds, st, w * 8 + 16, lane);
    panel_head<8, 1>(pp, bw);
    BAR();
    STW(5, 1, 384);
    const f32x4 cs = *(const f32x4*)(csum + (hg * 7 + 6) * 32 + g * 8 + 4 * t);
    f32x4 acc[5][1];
    __builtin_amdgcn_s_setprio(3);
    float* sf = (float*)st;
    const int j = lane & 15;
    if (t == 0) {
      f32x4 gr[3];
      wave_gemm<5, 8, 1, 3, 0>(pp, bw, lds, lane, acc, gr);
#pragma unroll
      for (int n = 0; n < 3; ++n) {
        if (g == 3) sf[(n * 16 + j) * 2] = acc[n][0][0];
        if (g == (j >> 2)) sf[(n * 16 + j) * 2 + 1] = (j & 2) ? ((j & 1) ? gr[n][3] : gr[n][2]) : ((j & 1) ? gr[n][1] : gr[n][0]);
      }
    } else {
      f32x4 gr[2];
      wave_gemm<5, 8, 1, 2, 3>(pp, bw, lds, lane, acc, gr);
#pragma unroll
      for (int n = 0; n < 2; ++n) {
        if (g == 3) sf[((n + 3) * 16 + j) * 2] = acc[n + 3][0][0];
        if (g == (j >> 2)) sf[((n + 3) * 16 + j) * 2 + 1] = (j & 2) ? ((j & 1) ? gr[n][3] : gr[n][2]) : ((j & 1) ? gr[n][1] : gr[n][0]);
      }
    }
    asm volatile("" ::: "memory");
    if (lane < (t == 0 ? 48 : 32)) st[t * 48 + lane] = sq2st(st[t * 48 + lane]);
    flag_set((unsigned*)(lds + FLOFF), t);
    __builtin_amdgcn_s_setprio(0);
    STW(5, 3, 384);
    flag_wait((unsigned*)(lds + FLOFF));
    if (g < 3) {
      u16* vt = (u16*)(lds + VOFF) + (48 + g * 8 + 4 * t) * VLD + (lane & 15);
      f32x2 sv[5];
#pragma unroll
      for (int m = 0; m < 5; ++m) sv[m] = st[m * 16 + (lane & 15)];
#pragma unroll
      for (int m = 0; m < 5; ++m) {
        f32x4 a = acc[m][0];
#pragma unroll
        for (int r = 0; r < 4; ++r) a[r] = (a[r] - sv[m][0] * cs[r]) * sv[m][1];
        put_vt(vt + m * 16, pk2(a[0], a[1])); put_vt(vt + m * 16 + 2 * VLD, pk2(a[2], a[3]));
      }
    }
  }
  STW(5, 4, 384); STW(6, 4, 256);
  BAR();
  STAMP(2, 4);
  {
    const int hh = w >> 1, j = lane & 15, g = lane >> 4, par = j & 1;
    const int LB = 16 + (w & 1) * 32;
    const unsigned* fp = tq + (LB - 16 + j) * QLD + hh * 12 + 4 * (g < 2 ? g : 2);
    u32x4 kf[3], qf[2];
#pragma unroll
    for (int n = 0; n < 3; ++n) kf[n] = *(const u32x4*)(fp + n * 16 * QLD + 48);
#pragma unroll
    for (int n = 0; n < 2; ++n) qf[n] = *(const u32x4*)(fp + (n + 1) * 16 * QLD);
    const u16* vt = (const u16*)(lds + VOFF) + LB - 16 + 4 * g;
    typedef __attribute__((ext_vector_type(2))) unsigned u32x2;
    u32x2 v0[3], v1[3];
#pragma unroll
    for (int n = 0; n < 3; ++n) {
      v0[n] = *(const u32x2*)(vt + (hh * 18 + j) * VLD + n * 16);
      v1[n] = *(const u32x2*)(vt + (hh * 18 + 16 + (j & 1)) * VLD + n * 16);
      if ((j & 3) == 2) v1[n] = (u32x2){0x3F803F80u, 0x3F803F80u};
    }
#pragma unroll
    for (int n = 0; n < 3; ++n) {
      if (g >= 2) { kf[n][1] = 0u; kf[n][2] = 0u; kf[n][3] = 0u; }
      if (g == 3) kf[n][0] = 0u;
    }
#pragma unroll
    for (int n = 0; n < 2; ++n) {
      if (g >= 2) { qf[n][1] = 0u; qf[n][2] = 0u; qf[n][3] = 0u; }
      if (g == 3) qf[n][0] = 0u;
    }
    const f32x4 z = {0.f, 0.f, 0.f, 0.f};
    const unsigned pm = par ? 0xffff0000u : 0x0000ffffu;
    const int dB = j - 4 * g - par;
#pragma unroll
    for (int n = 0; n < 2; ++n) {
      const int grow = rt * 64 + LB - 16 + n * 16 + j;
      const unsigned lim = grow < 14 ? grow : 14;
      const f32x4 sa = MFMA16(BF(kf[n]), BF(qf[n]), z), sb = MFMA16(BF(kf[n + 1]), BF(qf[n]), z);
      float e[4] = {par ? sa[1] : sa[0], par ? sa[3] : sa[2], par ? sb[1] : sb[0], par ? sb[3] : sb[2]};
      if ((unsigned)(dB + 16) > lim) e[0] = -3.0e38f;
      if ((unsigned)(dB + 14) > lim) e[1] = -3.0e38f;
      if ((unsigned)dB > lim) e[2] = -3.0e38f;
      if ((unsigned)(dB - 2) > lim) e[3] = -3.0e38f;
      const float mx = xrow_max(fmaxf(fmaxf(e[0], e[1]), fmaxf(e[2], e[3])));
      constexpr float cc = 0.23570226039551584f * 1.4426950408889634f;
      u32x4 pf;
#pragma unroll
      for (int i = 0; i < 4; ++i) { const float pv = __builtin_amdgcn_exp2f((e[i] - mx) * cc); pf[i] = pk2(pv, pv) & pm; }
      const u32x4 a0 = {v0[n][0], v0[n][1], v0[n + 1][0], v0[n + 1][1]};
      const u32x4 a1 = {v1[n][0], v1[n][1], v1[n + 1][0], v1[n + 1][1]};
      const f32x4 o0 = MFMA16(BF(a0), BF(pf), z), o1 = MFMA16(BF(a1), BF(pf), z);
      const float inv = 1.f / o1[2];
      unsigned* op = (unsigned*)(o + (size_t)grow * D_ + (hg * 4 + hh) * 18);
      op[2 * g] = pk2(o0[0] * inv, o0[1] * inv);
      op[2 * g + 1] = pk2(o0[2] * inv, o0[3] * inv);
      if (g == 0) op[8] = pk2(o1[0] * inv, o1[1] * inv);
    }
  }
  STAMP(2, 7);
}

__device__ __forceinline__ float xhalf_max(float m) {
  auto rr = __builtin_amdgcn_permlane32_swap(__float_as_uint(m), __float_as_uint(m), false, false);
  return fmaxf(__uint_as_float(rr[0]), __uint_as_float(rr[1]));
}
#define EX2(x) __builtin_amdgcn_exp2f(x)
#define PMODE 1
#define SP(n) __builtin_amdgcn_s_setprio(n)
#define PRIO(it) { switch ((it) & 3) { case 0: SP(3); break; case 1: SP(2); break; case 2: SP(1); break; default: SP(0); } }
__global__ __launch_bounds__(512, 2) void attn_kernel(const u16* __restrict__ q, const u32x4* __restrict__ kv,
                                                      u16* __restrict__ o) {
  extern __shared__ __attribute__((aligned(16))) char lds[];
  const int tid = threadIdx.x, lane = tid & 63, w = tid >> 6;
  const int r = lane & 31, h = lane >> 5;
  const int qt = blockIdx.x >> 2, head = (blockIdx.x & 3) * 8 + w;
  STAMP(0, 0);
  constexpr int CH = 16 * KV_REC / 16;
  const int c4 = tid + 2048 < CH ? tid + 2048 : CH - 1;
  u32x4 tt[5];
#pragma unroll
  for (int i = 0; i < 4; ++i) tt[i] = kv[tid + i * 512];
  tt[4] = kv[c4];
  const int qrow = qt * 32 + r;
  const unsigned* qp = (const unsigned*)(q + (size_t)qrow * D_ + head * 18);
  u32x4 qlo_u, qhi_u = {0u, 0u, 0u, 0u};
#pragma unroll
  for (int i = 0; i < 4; ++i) qlo_u[i] = qp[h * 4 + i];
  if (h == 0) qhi_u[0] = qp[8];
  u32x4* l4 = (u32x4*)lds;
#define PUTCH(C) { _Pragma("unroll") for (int i = 0; i < 4; ++i) l4[(C) * CH + tid + i * 512] = tt[i]; l4[(C) * CH + c4] = tt[4]; }
  PUTCH(0);
  const bf16x8 qlo = __builtin_bit_cast(bf16x8, qlo_u), qhi = __builtin_bit_cast(bf16x8, qhi_u);
  STAMP(0, 1);
  BAR();
  STAMP(0, 2);
  const char* kloP = lds + r * 32 + h * 16;
  const char* khiP = lds + KV_KHI + r * 4;
  const int dcl = r < 19 ? r : 18;
  const char* vP = lds + KV_VT + h * 304 + dcl * 16;
  f32x16 negm, acc, sA, sB;
  bf16x8 kA, kB, vA0, vA1, vB0, vB1;
  u32x4 hA = {0u, 0u, 0u, 0u}, hB = {0u, 0u, 0u, 0u};
  u32x4 pA0, pA1, pB0 = {0u, 0u, 0u, 0u}, pB1 = {0u, 0u, 0u, 0u};
#pragma unroll
  for (int i = 0; i < 16; ++i) { negm[i] = 0.f; acc[i] = 0.f; }
  kA = *(const bf16x8*)kloP; hA[0] = *(const unsigned*)khiP;
  kB = *(const bf16x8*)(kloP + KV_REC); hB[0] = *(const unsigned*)(khiP + KV_REC);
  {
    sA = MFMA32(kA, qlo, negm);
    sA = MFMA32(BF(hA), qhi, sA);
    float tm = sA[0];
#pragma unroll
    for (int i = 1; i < 16; ++i) tm = fmaxf(tm, sA[i]);
    tm = xhalf_max(tm);
#pragma unroll
    for (int i = 0; i < 16; ++i) { negm[i] = -tm; sA[i] -= tm; }
  }
  vB0 = BF(pB0); vB1 = BF(pB0);
#define E4(SS, PD, I0, J0) { PD[J0] = pk2(EX2(SS[I0]), EX2(SS[I0 + 1])); PD[J0 + 1] = pk2(EX2(SS[I0 + 2]), EX2(SS[I0 + 3])); }
#define STEP(OFF, SC, SN, KLN, KHN, KL2, KH2, VP0, VP1, VC0, VC1, PP0, PP1, PC0, PC1) { \
    SN = MFMA32(KLN, qlo, negm); \
    KL2 = *(const bf16x8*)(kloP + (OFF) + 2 * KV_REC); KH2[0] = *(const unsigned*)(khiP + (OFF) + 2 * KV_REC); \
    SB; E4(SC, PC0, 0, 0); SB; \
    acc = MFMA32(VP0, BF(PP0), acc); \
    VC0 = *(const bf16x8*)(vP + (OFF)); VC1 = *(const bf16x8*)(vP + (OFF) + 608); \
    SB; E4(SC, PC0, 4, 2); SB; \
    SN = MFMA32(BF(KHN), qhi, SN); \
    SB; E4(SC, PC1, 8, 0); SB; \
    acc = MFMA32(VP1, BF(PP1), acc); \
    SB; E4(SC, PC1, 12, 2); SB; }
  asm volatile("" : "+v"(hA), "+v"(hB));
#define CHUNK(NEXT) { \
    _Pragma("unroll") for (int it = 0; it < 8; ++it) { \
      if (NEXT && it == 7) { PUTCH(c + 1); BAR(); } \
      PRIO(it); \
      if (NEXT && it < 2) tt[2 * it] = kn[tid + 2 * it * 512]; \
      if (NEXT && it == 2) tt[4] = kn[c4]; \
      STEP(2 * it * KV_REC, sA, sB, kB, hB, kA, hA, vB0, vB1, vA0, vA1, pB0, pB1, pA0, pA1); \
      if (NEXT && it < 2) tt[2 * it + 1] = kn[tid + (2 * it + 1) * 512]; \
      STEP((2 * it + 1) * KV_REC, sB, sA, kA, hA, kB, hB, vA0, vA1, vB0, vB1, pA0, pA1, pB0, pB1); \
    } \
    kloP += 16 * KV_REC; khiP += 16 * KV_REC; vP += 16 * KV_REC; }
  for (int c = 0; c < 3; ++c) {
    const u32x4* kn = kv + (c + 1) * CH;
    CHUNK(true);
  }
  {
    constexpr int c = 3; const u32x4* kn = kv;
    CHUNK(false);
  }
  acc = MFMA32(vB0, BF(pB0), acc);
  acc = MFMA32(vB1, BF(pB1), acc);
  STAMP(0, 3);
  {
    float chk = 0.f;
#pragma unroll
    for (int i = 0; i < 11; ++i) chk += acc[i] * 0.f;
    if (__builtin_expect(__any(!(chk == 0.f)), 0)) {
      kloP = lds + r * 32 + h * 16; khiP = lds + KV_KHI + r * 4; vP = lds + KV_VT + h * 304 + dcl * 16;
      float m = -3.0e38f;
#pragma unroll
      for (int i = 0; i < 16; ++i) { acc[i] = 0.f; negm[i] = 0.f; }
      for (int t = 0; t < 64; ++t) {
        kA = *(const bf16x8*)(kloP + t * KV_REC); hA[0] = *(const unsigned*)(khiP + t * KV_REC);
        vA0 = *(const bf16x8*)(vP + t * KV_REC); vA1 = *(const bf16x8*)(vP + t * KV_REC + 608);
        sA = MFMA32(kA, qlo, negm);
        sA = MFMA32(BF(hA), qhi, sA);
        float tm = sA[0];
#pragma unroll
        for (int i = 1; i < 16; ++i) tm = fmaxf(tm, sA[i]);
        tm = fmaxf(xhalf_max(tm), m);
        const float al = EX2(m - tm);
        m = tm;
#pragma unroll
        for (int i = 0; i < 16; ++i) { acc[i] *= al; sA[i] -= tm; }
        E4(sA, pA0, 0, 0); E4(sA, pA0, 4, 2); E4(sA, pA1, 8, 0); E4(sA, pA1, 12, 2);
        acc = MFMA32(vA0, BF(pA0), acc);
        acc = MFMA32(vA1, BF(pA1), acc);
      }
    }
  }
  auto rr = __builtin_amdgcn_permlane32_swap(__float_as_uint(acc[10]), __float_as_uint(acc[10]), false, false);
  const float l = __uint_as_float(rr[0]);
  const float inv = 1.f / l;
  unsigned* op = (unsigned*)(o + (size_t)qrow * D_ + head * 18);
  op[2 * h + 0] = pk2(acc[0] * inv, acc[1] * inv);
  op[2 * h + 1] = pk2(acc[2] * inv, acc[3] * inv);
  op[4 + 2 * h + 0] = pk2(acc[4] * inv, acc[5] * inv);
  op[4 + 2 * h + 1] = pk2(acc[6] * inv, acc[7] * inv);
  if (h == 0) op[8] = pk2(acc[8] * inv, acc[9] * inv);
  STAMP(0, 7);
}

extern "C" void kernel_launch(void* const* d_in, const int* in_sizes, int n_in, void* d_out, int out_size,
                              void* d_ws, size_t ws_size, hipStream_t stream) {
  const u16* x0 = (const u16*)d_in[0];
  const u16 *wq = (const u16*)d_in[1], *wk = (const u16*)d_in[2], *wv = (const u16*)d_in[3];
  const u16 *woa = (const u16*)d_in[4], *wqkv = (const u16*)d_in[6], *wob = (const u16*)d_in[7];
  char* ws = (char*)d_ws;
  u16* xs[5] = {(u16*)x0, (u16*)(ws + OFF_X1), (u16*)(ws + OFF_X2), (u16*)(ws + OFF_X3), (u16*)d_out};
  u16* qb = (u16*)(ws + OFF_Q);
  u16* ob = (u16*)(ws + OFF_O);
  char* kv = ws + OFF_KV;
  const float* cs = (const float*)(ws + OFF_CS);
  (void)hipFuncSetAttribute((const void*)attn_kernel, hipFuncAttributeMaxDynamicSharedMemorySize, ATT_LDS);
  (void)hipFuncSetAttribute((const void*)ln_qkv_a_kernel, hipFuncAttributeMaxDynamicSharedMemorySize, QA_LDS);
  (void)hipFuncSetAttribute((const void*)proj_res_kernel, hipFuncAttributeMaxDynamicSharedMemorySize, 64 * LDA);
  (void)hipFuncSetAttribute((const void*)ln_qkv_local_kernel, hipFuncAttributeMaxDynamicSharedMemorySize, LOC_LDS);
  prep_kernel<<<112, 576, 0, stream>>>(wq, wk, wv, woa, wqkv, wob, (u32x4*)ws, (float*)(ws + OFF_CS));
  for (int l = 0; l < 2; ++l) {
    ln_qkv_a_kernel<<<224, 512, QA_LDS, stream>>>(xs[2 * l], (const u32x4*)(ws + OFF_WQKVA), cs, qb, kv);
    attn_kernel<<<256, 512, ATT_LDS, stream>>>(qb, (const u32x4*)kv, ob);
    proj_res_kernel<<<192, 512, 64 * LDA, stream>>>(ob, (const u32x4*)(ws + OFF_WOA), xs[2 * l], xs[2 * l + 1]);
    ln_qkv_local_kernel<<<256, 512, LOC_LDS, stream>>>(xs[2 * l + 1], (const u32x4*)(ws + OFF_WQKVB), cs + 38 * 32, ob);
    proj_res_kernel<<<192, 512, 64 * LDA, stream>>>(ob, (const u32x4*)(ws + OFF_WOB), xs[2 * l + 1], xs[2 * l + 2]);
  }
}
```

```cpp
#include <hip/hip_runtime.h>
#include <hip/hip_bf16.h>
#include <stdint.h>

typedef __attribute__((ext_vector_type(8))) short bf16x8;
typedef __attribute__((ext_vector_type(4))) float f32x4;
typedef __attribute__((ext_vector_type(2))) float f32x2;
typedef __attribute__((ext_vector_type(16))) float f32x16;
typedef __attribute__((ext_vector_type(4))) unsigned u32x4;
typedef __attribute__((ext_vector_type(2))) __bf16 bf16x2_t;
typedef unsigned short u16;

constexpr int S_ = 2048, D_ = 576;
constexpr int KS = 18;
constexpr int LDA = 1168;
constexpr int PANEL_CH = 2 * KS * 64;

constexpr size_t PANEL_B = (size_t)PANEL_CH * 16;
constexpr size_t OFF_WQKVA = 0;
constexpr size_t OFF_WOA = OFF_WQKVA + 20 * PANEL_B;
constexpr size_t OFF_WQKVB = OFF_WOA + 18 * PANEL_B;
constexpr size_t OFF_WOB = OFF_WQKVB + 56 * PANEL_B;
constexpr size_t ACT_B = (size_t)S_ * D_ * 2;
constexpr size_t OFF_Q = OFF_WOB + 18 * PANEL_B;
constexpr size_t OFF_O = OFF_Q + ACT_B;
constexpr size_t OFF_X1 = OFF_O + ACT_B;
constexpr size_t OFF_X2 = OFF_X1 + ACT_B;
constexpr size_t OFF_X3 = OFF_X2 + ACT_B;
constexpr size_t OFF_KV = OFF_X3 + ACT_B;
constexpr size_t OFF_CS = OFF_KV + 262144;
constexpr int KV_REC = 2368, KV_KHI = 1024, KV_VT = 1152;
constexpr int KV_BYTES = 64 * KV_REC;
constexpr int ATT_LDS = 66 * KV_REC;

#define STAMP(K, I)
#define STW(K, I, T)
__device__ __forceinline__ unsigned pk2(float a, float b) {
  f32x2 v = {a, b};
  bf16x2_t r = __builtin_convertvector(v, bf16x2_t);
  return __builtin_bit_cast(unsigned, r);
}
__device__ __forceinline__ float bflo(unsigned u) { return __uint_as_float(u << 16); }
__device__ __forceinline__ float bfhi(unsigned u) { return __uint_as_float(u & 0xffff0000u); }
__device__ __forceinline__ float rbf(float a) { return bflo(pk2(a, 0.f) & 0xffffu); }

__global__ __launch_bounds__(576) void prep_kernel(const u16* __restrict__ wq, const u16* __restrict__ wk,
                                                   const u16* __restrict__ wv, const u16* __restrict__ woa,
                                                   const u16* __restrict__ wqkv, const u16* __restrict__ wob,
                                                   u32x4* __restrict__ out, float* __restrict__ cs) {
  __shared__ __attribute__((aligned(16))) u16 tile[576 * 40];
  __shared__ float red[9][32];
  const int panel = blockIdx.x, tid = threadIdx.x, lane = tid & 63, w = tid >> 6;
  STAMP(4, 0);
  if (panel == 18 || panel == 19) {
    const u16* src = panel == 18 ? wk : wv;
#pragma unroll
    for (int c = 0; c < 32; ++c) tile[tid * 40 + c] = c < 18 ? src[tid * 18 + c] : (u16)0;
  } else {
    const int c8 = (tid & 3) * 8;
    const u16* src; int ld, col; bool ok = true;
    if (panel < 18) { src = wq; ld = 576; col = 32 * panel + c8; }
    else if (panel < 38) { src = woa; ld = 576; col = 32 * (panel - 20) + c8; }
    else if (panel < 94) {
      const int p = panel - 38, g = p / 7, cc = 32 * (p % 7) + c8;
      src = wqkv; ld = 1728; ok = cc < 216; col = ok ? (cc / 72) * 576 + g * 72 + (cc % 72) : 0;
    } else { src = wob; ld = 576; col = 32 * (panel - 94) + c8; }
    u32x4 v[4];
#pragma unroll
    for (int i = 0; i < 4; ++i) v[i] = *(const u32x4*)(src + (size_t)((tid >> 2) + 144 * i) * ld + col);
#pragma unroll
    for (int i = 0; i < 4; ++i) {
      if (!ok) v[i] = (u32x4){0x3F80u, 0u, 0x3F80u, 0u};
      *(u32x4*)(tile + ((tid >> 2) + 144 * i) * 40 + c8) = v[i];
    }
  }
  STAMP(4, 1);
  __syncthreads();
  STAMP(4, 2);
  const int i = lane & 15;
#pragma unroll
  for (int t = 0; t < 2; ++t) {
    const int c = 8 * (i >> 2) + 4 * t + (i & 3);
    float sum = 0.f;
#pragma unroll
    for (int h = 0; h < 2; ++h) {
      const int ks = w + 9 * h;
      const u16* tp = tile + (32 * ks + 8 * (lane >> 4)) * 40 + c;
      u32x4 v;
#pragma unroll
      for (int j = 0; j < 4; ++j) {
        const unsigned lo = tp[(2 * j) * 40], hi = tp[(2 * j + 1) * 40];
        v[j] = lo | (hi << 16);
        sum += bflo(v[j]) + bfhi(v[j]);
      }
      out[(size_t)panel * PANEL_CH + (t * KS + ks) * 64 + lane] = v;
    }
    sum += __shfl_xor(sum, 16); sum += __shfl_xor(sum, 32);
    if (lane < 16) red[w][c] = sum;
  }
  STAMP(4, 3);
  __syncthreads();
  if (tid < 32) {
    float sum = 0.f;
#pragma unroll
    for (int k = 0; k < 9; ++k) sum += red[k][tid];
    cs[panel * 32 + tid] = sum;
  }
  STAMP(4, 7);
}

__device__ __forceinline__ void load8(const u16* __restrict__ src, int grow0, int lane, u32x4 (&v)[9]) {
  int grow = grow0 + (lane >> 3); grow = grow < 0 ? 0 : grow;
  const u32x4* p = (const u32x4*)(src + (size_t)grow * D_) + (lane & 7);
#pragma unroll
  for (int i = 0; i < 9; ++i) v[i] = p[8 * i];
}
__device__ __forceinline__ float dot2(unsigned a, unsigned b, float c) {
  return __builtin_amdgcn_fdot2_f32_bf16(__builtin_bit_cast(bf16x2_t, a), __builtin_bit_cast(bf16x2_t, b), c, false);
}
template <int C>
__device__ __forceinline__ float dppf(float v) {
  return __int_as_float(__builtin_amdgcn_update_dpp(0, __float_as_int(v), C, 0xf, 0xf, true));
}
__device__ __forceinline__ void stat_acc(const u32x4& v, f32x4& a) {
#pragma unroll
  for (int j = 0; j < 4; j += 2) {
    a[0] = dot2(v[j], 0x3F803F80u, a[0]); a[1] = dot2(v[j + 1], 0x3F803F80u, a[1]);
    a[2] = dot2(v[j], v[j], a[2]); a[3] = dot2(v[j + 1], v[j + 1], a[3]);
  }
}
__device__ __forceinline__ void stat_fin(const f32x4& a, f32x2* st, int lr0, int lane) {
  float s = a[0] + a[1], q = a[2] + a[3];
  s += dppf<0xB1>(s); q += dppf<0xB1>(q);
  s += dppf<0x4E>(s); q += dppf<0x4E>(q);
  s += dppf<0x141>(s); q += dppf<0x141>(q);
  const float mean = s * (1.f / 576.f);
  const float var = fmaxf(q * (1.f / 576.f) - mean * mean, 0.f);
  st[lr0 + (lane >> 3)] = (f32x2){mean, rsqrtf(var + 1e-5f)};
}
__device__ __forceinline__ void stat8(const u32x4 (&v)[9], f32x2* st, int lr0, int lane) {
  f32x4 a = {0.f, 0.f, 0.f, 0.f};
#pragma unroll
  for (int i = 0; i < 9; ++i) stat_acc(v[i], a);
  stat_fin(a, st, lr0, lane);
}
typedef __attribute__((address_space(3))) volatile unsigned lds_vu32;
__device__ __forceinline__ void flag_set(unsigned* fl, int w) {
  asm volatile("" ::: "memory");
  ((lds_vu32*)fl)[w] = 1u;
  asm volatile("" ::: "memory");
}
__device__ __forceinline__ void flag_wait(unsigned* fl) {
  while (true) {
    asm volatile("" ::: "memory");
    const unsigned a = ((lds_vu32*)fl)[0], b = ((lds_vu32*)fl)[1];
    if ((a & b) != 0u) break;
    __builtin_amdgcn_s_sleep(1);
  }
  asm volatile("" ::: "memory");
}
__device__ __forceinline__ f32x2 sq2st(f32x2 sq) {
  const float mean = sq[0] * (1.f / 576.f);
  const float var = fmaxf(sq[1] * (1.f / 576.f) - mean * mean, 0.f);
  return (f32x2){mean, rsqrtf(var + 1e-5f)};
}
template <bool LN>
__device__ __forceinline__ void put8(u32x4 (&v)[9], char* lds, f32x2* st, int lr0, int lane) {
  char* d = lds + (lr0 + (lane >> 3)) * LDA + (lane & 7) * 16;
#pragma unroll
  for (int i = 0; i < 9; ++i) *(u32x4*)(d + 128 * i) = v[i];
  if (LN) stat8(v, st, lr0, lane);
}

#define MFMA16(a, b, c) __builtin_amdgcn_mfma_f32_16x16x32_bf16(a, b, c, 0, 0, 0)
#define MFMA32(a, b, c) __builtin_amdgcn_mfma_f32_32x32x16_bf16(a, b, c, 0, 0, 0)
#define SB __builtin_amdgcn_sched_barrier(0)
#define BAR0() asm volatile("s_barrier" ::: "memory")
#define BAR() asm volatile("s_waitcnt lgkmcnt(0)\n\ts_barrier" ::: "memory")

template <int PRE, int NT = 2>
__device__ __forceinline__ void panel_head(const u32x4* __restrict__ p, bf16x8 (&b)[NT][KS]) {
#pragma unroll
  for (int ks = 0; ks < PRE; ++ks) {
#pragma unroll
    for (int t = 0; t < NT; ++t) b[t][ks] = __builtin_bit_cast(bf16x8, p[(t * KS + ks) * 64]);
    SB;
  }
}
template <int MT, int PRE, int NT = 2, int NG = 0, int G0 = 0>
__device__ __forceinline__ void wave_gemm(const u32x4* __restrict__ p, bf16x8 (&b)[NT][KS], const char* lds, int lane,
                                          f32x4 (&acc)[MT][NT], f32x4* gr = nullptr) {
  const char* ap = lds + (lane & 15) * LDA + (lane >> 4) * 16;
  bf16x8 a[2][MT];
#pragma unroll
  for (int n = 0; n < NG; ++n) gr[n] = (f32x4){0.f, 0.f, 0.f, 0.f};
#pragma unroll
  for (int m = 0; m < MT; ++m) {
#pragma unroll
    for (int t = 0; t < NT; ++t) acc[m][t] = (f32x4){0.f, 0.f, 0.f, 0.f};
    a[0][m] = *(const bf16x8*)(ap + m * 16 * LDA);
  }
#pragma unroll
  for (int ks = 0; ks < KS; ++ks) {
    if (ks + PRE < KS) {
#pragma unroll
      for (int t = 0; t < NT; ++t) b[t][ks + PRE] = __builtin_bit_cast(bf16x8, p[(t * KS + ks + PRE) * 64]);
    }
    if (ks + 1 < KS) {
#pragma unroll
      for (int m = 0; m < MT; ++m) a[(ks + 1) & 1][m] = *(const bf16x8*)(ap + m * 16 * LDA + (ks + 1) * 64);
    }
    SB;
#pragma unroll
    for (int m = 0; m < MT; ++m)
#pragma unroll
      for (int t = 0; t < NT; ++t) acc[m][t] = MFMA16(b[t][ks], a[ks & 1][m], acc[m][t]);
#pragma unroll
    for (int n = 0; n < NG; ++n) gr[n] = MFMA16(a[ks & 1][G0 + n], a[ks & 1][G0 + n], gr[n]);
    SB;
  }
#pragma unroll
  for (int m = 0; m < MT; ++m)
#pragma unroll
    for (int t = 0; t < NT; ++t) asm volatile("" : "+v"(acc[m][t]));
#pragma unroll
  for (int n = 0; n < NG; ++n) asm volatile("" : "+v"(gr[n]));
}
__device__ __forceinline__ void ln_fix(f32x4 (&acc)[2], const f32x4 (&cs)[2], f32x2 st, float scale) {
  const float rs = st[1] * scale;
#pragma unroll
  for (int t = 0; t < 2; ++t)
#pragma unroll
    for (int r = 0; r < 4; ++r) acc[t][r] = (acc[t][r] - st[0] * cs[t][r]) * rs;
}

constexpr int QA_LDS = 64 * LDA + 64 * 8;
__global__ __launch_bounds__(512) void ln_qkv_a_kernel(const u16* __restrict__ x, const u32x4* __restrict__ wp,
                                                       const float* __restrict__ csum, u16* __restrict__ q,
                                                       char* __restrict__ kv) {
  extern __shared__ __attribute__((aligned(16))) char lds[];
  const int tid = threadIdx.x, lane = tid & 63, w = tid >> 6;
  const int b = blockIdx.x;
  const int rt = 4 * (b & 7) + (b >> 3) / 7;
  const int panel = 3 * ((b >> 3) % 7) + w;
  f32x2* st = (f32x2*)(lds + 64 * LDA);
  u32x4 v8[9];
  load8(x, rt * 64 + w * 8, lane, v8);
  SB;
  BAR0();
  if (!(w < 3 && panel < 20)) {
    put8<true>(v8, lds, st, w * 8, lane);
    BAR();
    return;
  }
  const int g = lane >> 4;
  bf16x8 bw[2][KS];
  const u32x4* pp = wp + (size_t)panel * PANEL_CH + lane;
  panel_head<8>(pp, bw);
  put8<true>(v8, lds, st, w * 8, lane);
  BAR();
  f32x4 cs[2];
  cs[0] = *(const f32x4*)(csum + panel * 32 + g * 8); cs[1] = *(const f32x4*)(csum + panel * 32 + g * 8 + 4);
  f32x4 acc[4][2];
  wave_gemm<4, 8>(pp, bw, lds, lane, acc);
#pragma unroll
  for (int m = 0; m < 4; ++m) {
    const int row = rt * 64 + m * 16 + (lane & 15);
    ln_fix(acc[m], cs, st[m * 16 + (lane & 15)], panel < 18 ? 0.23570226039551584f * 1.4426950408889634f : 1.f);
    u32x4 v;
    v[0] = pk2(acc[m][0][0], acc[m][0][1]); v[1] = pk2(acc[m][0][2], acc[m][0][3]);
    v[2] = pk2(acc[m][1][0], acc[m][1][1]); v[3] = pk2(acc[m][1][2], acc[m][1][3]);
    if (panel < 18) {
      *(u32x4*)(q + (size_t)row * D_ + panel * 32 + g * 8) = v;
    } else if (panel == 18) {
      char* rec = kv + (row >> 5) * KV_REC;
      if (g < 2) *(u32x4*)(rec + (row & 31) * 32 + g * 16) = v;
      else if (g == 2) *(unsigned*)(rec + KV_KHI + (row & 31) * 4) = v[0];
    } else {
      const int T = row >> 5, kk = row & 31;
      const int s = kk >> 4, h = (kk >> 2) & 1, j = ((kk >> 3) & 1) * 4 + (kk & 3);
      u16* vb = (u16*)(kv + T * KV_REC + KV_VT + ((s * 2 + h) * 19) * 16 + j * 2);
#pragma unroll
      for (int e = 0; e < 8; ++e) {
        const int d = 8 * g + e;
        if (d < 18) vb[d * 8] = (u16)((v[e >> 1] >> ((e & 1) * 16)) & 0xffffu);
        else if (d == 18) vb[d * 8] = (u16)0x3F80;
      }
    }
  }
}

__global__ __launch_bounds__(512) void proj_res_kernel(const u16* __restrict__ o, const u32x4* __restrict__ wp,
                                                       const u16* __restrict__ xin, u16* __restrict__ xout) {
  extern __shared__ __attribute__((aligned(16))) char lds[];
  const int tid = threadIdx.x, lane = tid & 63, w = tid >> 6;
  const int b = blockIdx.x;
  const int rt = 4 * (b & 7) + (b >> 3) / 6, panel = 3 * ((b >> 3) % 6) + w;
  const int g = lane >> 4;
  STAMP(1, 0);
  u32x4 v8[9];
  load8(o, rt * 64 + w * 8, lane, v8);
  SB;
  BAR0();
  if (w >= 3) {
    put8<false>(v8, lds, nullptr, w * 8, lane);
    BAR();
    return;
  }
  bf16x8 bw[2][KS];
  u32x4 xr[4];
  const u32x4* pp = wp + (size_t)panel * PANEL_CH + lane;
  panel_head<8>(pp, bw);
  put8<false>(v8, lds, nullptr, w * 8, lane);
  STAMP(1, 1);
  BAR();
  STAMP(1, 2);
#pragma unroll
  for (int m = 0; m < 4; ++m) {
    const int row = rt * 64 + m * 16 + (lane & 15);
    xr[m] = *(const u32x4*)(xin + (size_t)row * D_ + panel * 32 + g * 8);
  }
  f32x4 acc[4][2];
  wave_gemm<4, 8>(pp, bw, lds, lane, acc);
  STAMP(1, 3);
#pragma unroll
  for (int m = 0; m < 4; ++m) {
    const int row = rt * 64 + m * 16 + (lane & 15);
    u32x4 v;
#pragma unroll
    for (int j = 0; j < 4; ++j)
      v[j] = pk2(bflo(xr[m][j]) + acc[m][j >> 1][(j & 1) * 2], bfhi(xr[m][j]) + acc[m][j >> 1][(j & 1) * 2 + 1]);
    *(u32x4*)(xout + (size_t)row * D_ + panel * 32 + g * 8) = v;
  }
  STAMP(1, 7);
}

constexpr int QLD = 148;
constexpr int QOFF = 80 * LDA;
constexpr int STOFF = QOFF + 80 * QLD * 4;
constexpr int FLOFF = STOFF + 80 * 8;
constexpr int LOC_LDS = FLOFF + 32;
__device__ __forceinline__ int segoff(int dw) { return (dw / 9) * 12 + dw % 9; }
template <int MT>
__device__ __forceinline__ void loc_full(const u32x4* __restrict__ pp, bf16x8 (&bw)[2][KS], const float* __restrict__ csp,
                                         char* lds, int lane, int pl) {
  constexpr int M0 = 5 - MT;
  const int g = lane >> 4;
  f32x4 cs[2];
  cs[0] = *(const f32x4*)(csp + g * 8); cs[1] = *(const f32x4*)(csp + g * 8 + 4);
  f32x4 acc[MT][2];
  f32x2* st = (f32x2*)(lds + STOFF);
  unsigned* tq = (unsigned*)(lds + QOFF) + (M0 * 16 + (lane & 15)) * QLD;
  int so[4];
#pragma unroll
  for (int j = 0; j < 4; ++j) { so[j] = segoff(pl * 16 + g * 4 + j); asm volatile("" : "+v"(so[j])); }
  wave_gemm<MT, 4>(pp, bw, lds + M0 * 16 * LDA, lane, acc);
  STAMP(2, 3); STW(6, 3, 256);
  flag_wait((unsigned*)(lds + FLOFF));
  f32x2 sv[MT];
#pragma unroll
  for (int m = 0; m < MT; ++m) sv[m] = st[(M0 + m) * 16 + (lane & 15)];
#pragma unroll
  for (int m = 0; m < MT; ++m) {
    ln_fix(acc[m], cs, sv[m], 1.f);
    unsigned* d = tq + m * 16 * QLD;
    d[so[0]] = pk2(acc[m][0][0], acc[m][0][1]); d[so[1]] = pk2(acc[m][0][2], acc[m][0][3]);
    d[so[2]] = pk2(acc[m][1][0], acc[m][1][1]); d[so[3]] = pk2(acc[m][1][2], acc[m][1][3]);
  }
}
__global__ __launch_bounds__(512) void ln_qkv_local_kernel(const u16* __restrict__ x, const u32x4* __restrict__ wp,
                                                           const float* __restrict__ csum, u16* __restrict__ o) {
  extern __shared__ __attribute__((aligned(16))) char lds[];
  const int tid = threadIdx.x, lane = tid & 63, w = tid >> 6;
  const int b = blockIdx.x;
  const int rt = 8 * ((b & 7) >> 1) + (b >> 5), hg = 4 * (b & 1) + ((b >> 3) & 3);
  const int r0 = rt * 64 - 16;
  unsigned* tq = (unsigned*)(lds + QOFF);
  f32x2* st = (f32x2*)(lds + STOFF);
  STAMP(2, 0); STW(5, 0, 384); STW(6, 0, 256);
  if (w < 6) {
    const int pl = w < 2 ? w : (w < 4 ? w + 2 : w - 2);
    u32x4 v8[1][9];
    load8(x, r0 + w * 8, lane, v8[0]);
    SB;
    BAR0();
    bf16x8 bw[2][KS];
    const u32x4* pp = wp + (size_t)(hg * 7 + pl) * PANEL_CH + lane;
    panel_head<4>(pp, bw);
    put8<false>(v8[0], lds, st, w * 8, lane);
    if (tid < 8) ((unsigned*)(lds + FLOFF))[tid] = 0u;
    STAMP(2, 1);
    BAR();
    STAMP(2, 2);
    if (w < 2) loc_full<4>(pp, bw, csum + (hg * 7 + pl) * 32, lds, lane, pl);
    else loc_full<5>(pp, bw, csum + (hg * 7 + pl) * 32, lds, lane, pl);
  } else {
    const int t = w - 6, g = lane >> 4;
    u32x4 v8[2][9];
    load8(x, r0 + w * 8, lane, v8[0]);
    load8(x, r0 + w * 8 + 16, lane, v8[1]);
    SB;
    BAR0();
    bf16x8 bw[1][KS];
    const u32x4* pp = wp + (size_t)(hg * 7 + 6) * PANEL_CH + t * KS * 64 + lane;
    put8<false>(v8[0], lds, st, w * 8, lane);
    put8<false>(v8[1], lds, st, w * 8 + 16, lane);
    panel_head<8, 1>(pp, bw);
    BAR();
    STW(5, 1, 384);
    const f32x4 cs = *(const f32x4*)(csum + (hg * 7 + 6) * 32 + g * 8 + 4 * t);
    f32x4 acc[5][1];
    __builtin_amdgcn_s_setprio(3);
    float* sf = (float*)st;
    const int j = lane & 15;
    if (t == 0) {
      f32x4 gr[3];
      wave_gemm<5, 8, 1, 3, 0>(pp, bw, lds, lane, acc, gr);
#pragma unroll
      for (int n = 0; n < 3; ++n) {
        if (g == 3) sf[(n * 16 + j) * 2] = acc[n][0][0];
        if (g == (j >> 2)) sf[(n * 16 + j) * 2 + 1] = (j & 2) ? ((j & 1) ? gr[n][3] : gr[n][2]) : ((j & 1) ? gr[n][1] : gr[n][0]);
      }
    } else {
      f32x4 gr[2];
      wave_gemm<5, 8, 1, 2, 3>(pp, bw, lds, lane, acc, gr);
#pragma unroll
      for (int n = 0; n < 2; ++n) {
        if (g == 3) sf[((n + 3) * 16 + j) * 2] = acc[n + 3][0][0];
        if (g == (j >> 2)) sf[((n + 3) * 16 + j) * 2 + 1] = (j & 2) ? ((j & 1) ? gr[n][3] : gr[n][2]) : ((j & 1) ? gr[n][1] : gr[n][0]);
      }
    }
    asm volatile("" ::: "memory");
    if (lane < (t == 0 ? 48 : 32)) st[t * 48 + lane] = sq2st(st[t * 48 + lane]);
    flag_set((unsigned*)(lds + FLOFF), t);
    __builtin_amdgcn_s_setprio(0);
    STW(5, 3, 384);
    flag_wait((unsigned*)(lds + FLOFF));
    if (g < 3) {
      const int s0 = segoff(96 + g * 4 + 2 * t), s1 = segoff(96 + g * 4 + 2 * t + 1);
      f32x2 sv[5];
#pragma unroll
      for (int m = 0; m < 5; ++m) sv[m] = st[m * 16 + (lane & 15)];
#pragma unroll
      for (int m = 0; m < 5; ++m) {
        f32x4 a = acc[m][0];
#pragma unroll
        for (int r = 0; r < 4; ++r) a[r] = (a[r] - sv[m][0] * cs[r]) * sv[m][1];
        unsigned* d = tq + (m * 16 + (lane & 15)) * QLD;
        d[s0] = pk2(a[0], a[1]); d[s1] = pk2(a[2], a[3]);
      }
    }
  }
  STW(5, 4, 384); STW(6, 4, 256);
  BAR();
  STAMP(2, 4);
  {
    const int hh = w >> 1, rl = (w & 1) * 32 + (lane & 31), sub = lane >> 5;
    const int grow = rt * 64 + rl, lrow = rl + 16;
    const unsigned* qp = tq + lrow * QLD + hh * 12;
    const u32x4 qa = *(const u32x4*)qp, qb = *(const u32x4*)(qp + 4);
    const unsigned qc = qp[8];
    float lg[8];
#pragma unroll
    for (int wi = 0; wi < 4; ++wi) {
      const int ww = sub * 4 + wi;
      const unsigned* kp = qp + 48 - 2 * ww * QLD;
      const u32x4 ka = *(const u32x4*)kp, kb = *(const u32x4*)(kp + 4);
      const unsigned kc = kp[8];
      float s0 = 0.f, s1 = 0.f;
#pragma unroll
      for (int i = 0; i < 4; ++i) { s0 = dot2(qa[i], ka[i], s0); s1 = dot2(qb[i], kb[i], s1); }
      s0 = dot2(qc, kc, s0);
      float s = (s0 + s1) * 0.23570226039551584f;
      s = (grow - 2 * ww >= 0) ? s : -1e9f;
      auto rr = __builtin_amdgcn_permlane32_swap(__float_as_uint(s), __float_as_uint(s), false, false);
      lg[wi] = __uint_as_float(rr[0]); lg[4 + wi] = __uint_as_float(rr[1]);
    }
    float mx = lg[0];
#pragma unroll
    for (int wi = 1; wi < 8; ++wi) mx = fmaxf(mx, lg[wi]);
    float sum = 0.f;
#pragma unroll
    for (int wi = 0; wi < 8; ++wi) { lg[wi] = __expf(lg[wi] - mx); sum += lg[wi]; }
    const float inv = 1.f / sum;
    float of[10];
#pragma unroll
    for (int i = 0; i < 10; ++i) of[i] = 0.f;
    const unsigned* vp0 = qp + 96 + sub * 4;
#pragma unroll
    for (int wi = 0; wi < 8; ++wi) {
      const unsigned* vp = vp0 - 2 * wi * QLD;
      const float p = lg[wi] * inv;
      const u32x4 u4 = *(const u32x4*)vp;
      const unsigned u5 = vp[4];
#pragma unroll
      for (int i = 0; i < 4; ++i) { of[2 * i] += p * bflo(u4[i]); of[2 * i + 1] += p * bfhi(u4[i]); }
      of[8] += p * bflo(u5); of[9] += p * bfhi(u5);
    }
    unsigned* op = (unsigned*)(o + (size_t)grow * D_ + (hg * 4 + hh) * 18) + sub * 4;
    if (sub == 0) op[0] = pk2(of[0], of[1]);
#pragma unroll
    for (int i = 1; i < 5; ++i) op[i] = pk2(of[2 * i], of[2 * i + 1]);
  }
  STAMP(2, 7);
}

__device__ __forceinline__ float xhalf_max(float m) {
  auto rr = __builtin_amdgcn_permlane32_swap(__float_as_uint(m), __float_as_uint(m), false, false);
  return fmaxf(__uint_as_float(rr[0]), __uint_as_float(rr[1]));
}
#define EX2(x) __builtin_amdgcn_exp2f(x)
#define PMODE 1
#define SP(n) __builtin_amdgcn_s_setprio(n)
#define PRIO(it) { switch ((it) & 3) { case 0: SP(3); break; case 1: SP(2); break; case 2: SP(1); break; default: SP(0); } }
__global__ __launch_bounds__(512, 2) void attn_kernel(const u16* __restrict__ q, const u32x4* __restrict__ kv,
                                                      u16* __restrict__ o) {
  extern __shared__ __attribute__((aligned(16))) char lds[];
  const int tid = threadIdx.x, lane = tid & 63, w = tid >> 6;
  const int r = lane & 31, h = lane >> 5;
  const int qt = blockIdx.x >> 2, head = (blockIdx.x & 3) * 8 + w;
  STAMP(0, 0);
  constexpr int CH = 16 * KV_REC / 16;
  const int c4 = tid + 2048 < CH ? tid + 2048 : CH - 1;
  u32x4 tt[5];
#pragma unroll
  for (int i = 0; i < 4; ++i) tt[i] = kv[tid + i * 512];
  tt[4] = kv[c4];
  const int qrow = qt * 32 + r;
  const unsigned* qp = (const unsigned*)(q + (size_t)qrow * D_ + head * 18);
  u32x4 qlo_u, qhi_u = {0u, 0u, 0u, 0u};
#pragma unroll
  for (int i = 0; i < 4; ++i) qlo_u[i] = qp[h * 4 + i];
  if (h == 0) qhi_u[0] = qp[8];
  u32x4* l4 = (u32x4*)lds;
#define PUTCH(C) { _Pragma("unroll") for (int i = 0; i < 4; ++i) l4[(C) * CH + tid + i * 512] = tt[i]; l4[(C) * CH + c4] = tt[4]; }
  PUTCH(0);
  const bf16x8 qlo = __builtin_bit_cast(bf16x8, qlo_u), qhi = __builtin_bit_cast(bf16x8, qhi_u);
  STAMP(0, 1);
  BAR();
  STAMP(0, 2);
  const char* kloP = lds + r * 32 + h * 16;
  const char* khiP = lds + KV_KHI + r * 4;
  const int dcl = r < 19 ? r : 18;
  const char* vP = lds + KV_VT + h * 304 + dcl * 16;
  f32x16 negm, acc, sA, sB;
  bf16x8 kA, kB, vA0, vA1, vB0, vB1;
  u32x4 hA = {0u, 0u, 0u, 0u}, hB = {0u, 0u, 0u, 0u};
  u32x4 pA0, pA1, pB0 = {0u, 0u, 0u, 0u}, pB1 = {0u, 0u, 0u, 0u};
#pragma unroll
  for (int i = 0; i < 16; ++i) { negm[i] = 0.f; acc[i] = 0.f; }
#define BF(x) __builtin_bit_cast(bf16x8, x)
  kA = *(const bf16x8*)kloP; hA[0] = *(const unsigned*)khiP;
  kB = *(const bf16x8*)(kloP + KV_REC); hB[0] = *(const unsigned*)(khiP + KV_REC);
  {
    sA = MFMA32(kA, qlo, negm);
    sA = MFMA32(BF(hA), qhi, sA);
    float tm = sA[0];
#pragma unroll
    for (int i = 1; i < 16; ++i) tm = fmaxf(tm, sA[i]);
    tm = xhalf_max(tm);
#pragma unroll
    for (int i = 0; i < 16; ++i) { negm[i] = -tm; sA[i] -= tm; }
  }
  vB0 = BF(pB0); vB1 = BF(pB0);
#define E4(SS, PD, I0, J0) { PD[J0] = pk2(EX2(SS[I0]), EX2(SS[I0 + 1])); PD[J0 + 1] = pk2(EX2(SS[I0 + 2]), EX2(SS[I0 + 3])); }
#define STEP(OFF, SC, SN, KLN, KHN, KL2, KH2, VP0, VP1, VC0, VC1, PP0, PP1, PC0, PC1) { \
    SN = MFMA32(KLN, qlo, negm); \
    KL2 = *(const bf16x8*)(kloP + (OFF) + 2 * KV_REC); KH2[0] = *(const unsigned*)(khiP + (OFF) + 2 * KV_REC); \
    SB; E4(SC, PC0, 0, 0); SB; \
    acc = MFMA32(VP0, BF(PP0), acc); \
    VC0 = *(const bf16x8*)(vP + (OFF)); VC1 = *(const bf16x8*)(vP + (OFF) + 608); \
    SB; E4(SC, PC0, 4, 2); SB; \
    SN = MFMA32(BF(KHN), qhi, SN); \
    SB; E4(SC, PC1, 8, 0); SB; \
    acc = MFMA32(VP1, BF(PP1), acc); \
    SB; E4(SC, PC1, 12, 2); SB; }
  asm volatile("" : "+v"(hA), "+v"(hB));
#define CHUNK(NEXT) { \
    _Pragma("unroll") for (int it = 0; it < 8; ++it) { \
      if (NEXT && it == 7) { PUTCH(c + 1); BAR(); } \
      PRIO(it); \
      if (NEXT && it < 2) tt[2 * it] = kn[tid + 2 * it * 512]; \
      if (NEXT && it == 2) tt[4] = kn[c4]; \
      STEP(2 * it * KV_REC, sA, sB, kB, hB, kA, hA, vB0, vB1, vA0, vA1, pB0, pB1, pA0, pA1); \
      if (NEXT && it < 2) tt[2 * it + 1] = kn[tid + (2 * it + 1) * 512]; \
      STEP((2 * it + 1) * KV_REC, sB, sA, kA, hA, kB, hB, vA0, vA1, vB0, vB1, pA0, pA1, pB0, pB1); \
    } \
    kloP += 16 * KV_REC; khiP += 16 * KV_REC; vP += 16 * KV_REC; }
  for (int c = 0; c < 3; ++c) {
    const u32x4* kn = kv + (c + 1) * CH;
    CHUNK(true);
  }
  {
    constexpr int c = 3; const u32x4* kn = kv;
    CHUNK(false);
  }
  acc = MFMA32(vB0, BF(pB0), acc);
  acc = MFMA32(vB1, BF(pB1), acc);
  STAMP(0, 3);
  {
    float chk = 0.f;
#pragma unroll
    for (int i = 0; i < 11; ++i) chk += acc[i] * 0.f;
    if (__builtin_expect(__any(!(chk == 0.f)), 0)) {
      kloP = lds + r * 32 + h * 16; khiP = lds + KV_KHI + r * 4; vP = lds + KV_VT + h * 304 + dcl * 16;
      float m = -3.0e38f;
#pragma unroll
      for (int i = 0; i < 16; ++i) { acc[i] = 0.f; negm[i] = 0.f; }
      for (int t = 0; t < 64; ++t) {
        kA = *(const bf16x8*)(kloP + t * KV_REC); hA[0] = *(const unsigned*)(khiP + t * KV_REC);
        vA0 = *(const bf16x8*)(vP + t * KV_REC); vA1 = *(const bf16x8*)(vP + t * KV_REC + 608);
        sA = MFMA32(kA, qlo, negm);
        sA = MFMA32(BF(hA), qhi, sA);
        float tm = sA[0];
#pragma unroll
        for (int i = 1; i < 16; ++i) tm = fmaxf(tm, sA[i]);
        tm = fmaxf(xhalf_max(tm), m);
        const float al = EX2(m - tm);
        m = tm;
#pragma unroll
        for (int i = 0; i < 16; ++i) { acc[i] *= al; sA[i] -= tm; }
        E4(sA, pA0, 0, 0); E4(sA, pA0, 4, 2); E4(sA, pA1, 8, 0); E4(sA, pA1, 12, 2);
        acc = MFMA32(vA0, BF(pA0), acc);
        acc = MFMA32(vA1, BF(pA1), acc);
      }
    }
  }
  auto rr = __builtin_amdgcn_permlane32_swap(__float_as_uint(acc[10]), __float_as_uint(acc[10]), false, false);
  const float l = __uint_as_float(rr[0]);
  const float inv = 1.f / l;
  unsigned* op = (unsigned*)(o + (size_t)qrow * D_ + head * 18);
  op[2 * h + 0] = pk2(acc[0] * inv, acc[1] * inv);
  op[2 * h + 1] = pk2(acc[2] * inv, acc[3] * inv);
  op[4 + 2 * h + 0] = pk2(acc[4] * inv, acc[5] * inv);
  op[4 + 2 * h + 1] = pk2(acc[6] * inv, acc[7] * inv);
  if (h == 0) op[8] = pk2(acc[8] * inv, acc[9] * inv);
  STAMP(0, 7);
}

extern "C" void kernel_launch(void* const* d_in, const int* in_sizes, int n_in, void* d_out, int out_size,
                              void* d_ws, size_t ws_size, hipStream_t stream) {
  const u16* x0 = (const u16*)d_in[0];
  const u16 *wq = (const u16*)d_in[1], *wk = (const u16*)d_in[2], *wv = (const u16*)d_in[3];
  const u16 *woa = (const u16*)d_in[4], *wqkv = (const u16*)d_in[6], *wob = (const u16*)d_in[7];
  char* ws = (char*)d_ws;
  u16* xs[5] = {(u16*)x0, (u16*)(ws + OFF_X1), (u16*)(ws + OFF_X2), (u16*)(ws + OFF_X3), (u16*)d_out};
  u16* qb = (u16*)(ws + OFF_Q);
  u16* ob = (u16*)(ws + OFF_O);
  char* kv = ws + OFF_KV;
  const float* cs = (const float*)(ws + OFF_CS);
  (void)hipFuncSetAttribute((const void*)attn_kernel, hipFuncAttributeMaxDynamicSharedMemorySize, ATT_LDS);
  (void)hipFuncSetAttribute((const void*)ln_qkv_a_kernel, hipFuncAttributeMaxDynamicSharedMemorySize, QA_LDS);
  (void)hipFuncSetAttribute((const void*)proj_res_kernel, hipFuncAttributeMaxDynamicSharedMemorySize, 64 * LDA);
  (void)hipFuncSetAttribute((const void*)ln_qkv_local_kernel, hipFuncAttributeMaxDynamicSharedMemorySize, LOC_LDS);
  prep_kernel<<<112, 576, 0, stream>>>(wq, wk, wv, woa, wqkv, wob, (u32x4*)ws, (float*)(ws + OFF_CS));
  for (int l = 0; l < 2; ++l) {
    ln_qkv_a_kernel<<<224, 512, QA_LDS, stream>>>(xs[2 * l], (const u32x4*)(ws + OFF_WQKVA), cs, qb, kv);
    attn_kernel<<<256, 512, ATT_LDS, stream>>>(qb, (const u32x4*)kv, ob);
    proj_res_kernel<<<192, 512, 64 * LDA, stream>>>(ob, (const u32x4*)(ws + OFF_WOA), xs[2 * l], xs[2 * l + 1]);
    ln_qkv_local_kernel<<<256, 512, LOC_LDS, stream>>>(xs[2 * l + 1], (const u32x4*)(ws + OFF_WQKVB), cs + 38 * 32, ob);
    proj_res_kernel<<<192, 512, 64 * LDA, stream>>>(ob, (const u32x4*)(ws + OFF_WOB), xs[2 * l + 1], xs[2 * l + 2]);
  }
}
```

```cpp
#include <hip/hip_runtime.h>
#include <hip/hip_bf16.h>
#include <stdint.h>

typedef __attribute__((ext_vector_type(8))) short bf16x8;
typedef __attribute__((ext_vector_type(4))) float f32x4;
typedef __attribute__((ext_vector_type(2))) float f32x2;
typedef __attribute__((ext_vector_type(16))) float f32x16;
typedef __attribute__((ext_vector_type(4))) unsigned u32x4;
typedef __attribute__((ext_vector_type(2))) __bf16 bf16x2_t;
typedef unsigned short u16;

constexpr int S_ = 2048, D_ = 576;
constexpr int KS = 18;
constexpr int LDA = 1168;
constexpr int PANEL_CH = 2 * KS * 64;

constexpr size_t PANEL_B = (size_t)PANEL_CH * 16;
constexpr size_t OFF_WQKVA = 0;
constexpr size_t OFF_WOA = OFF_WQKVA + 20 * PANEL_B;
constexpr size_t OFF_WQKVB = OFF_WOA + 18 * PANEL_B;
constexpr size_t OFF_WOB = OFF_WQKVB + 56 * PANEL_B;
constexpr size_t ACT_B = (size_t)S_ * D_ * 2;
constexpr size_t OFF_Q = OFF_WOB + 18 * PANEL_B;
constexpr size_t OFF_O = OFF_Q + ACT_B;
constexpr size_t OFF_X1 = OFF_O + ACT_B;
constexpr size_t OFF_X2 = OFF_X1 + ACT_B;
constexpr size_t OFF_X3 = OFF_X2 + ACT_B;
constexpr size_t OFF_KV = OFF_X3 + ACT_B;
constexpr size_t OFF_CS = OFF_KV + 262144;
constexpr size_t OFF_CSP = OFF_CS + 16384;
constexpr int KV_REC = 2368, KV_KHI = 1024, KV_VT = 1152;
constexpr int KV_BYTES = 64 * KV_REC;
constexpr int ATT_LDS = 66 * KV_REC;

#define STAMP(K, I)
#define STW(K, I, T)
__device__ __forceinline__ unsigned pk2(float a, float b) {
  f32x2 v = {a, b};
  bf16x2_t r = __builtin_convertvector(v, bf16x2_t);
  return __builtin_bit_cast(unsigned, r);
}
__device__ __forceinline__ float bflo(unsigned u) { return __uint_as_float(u << 16); }
__device__ __forceinline__ float bfhi(unsigned u) { return __uint_as_float(u & 0xffff0000u); }
__device__ __forceinline__ float rbf(float a) { return bflo(pk2(a, 0.f) & 0xffffu); }

__device__ __forceinline__ void wt16(void* base, unsigned nbytes, unsigned off, const u32x4& v) {
  __builtin_amdgcn_raw_buffer_store_b128(v, __builtin_amdgcn_make_buffer_rsrc(base, (short)0, (int)nbytes, 0x00020000),
                                         (int)off, 0, 16);
}
struct WSrc { const u16 *wq, *wk, *wv, *woa, *wqkv, *wob; };
__device__ __forceinline__ const unsigned* raw_src(const WSrc& ws, int panel, int u, int& ldw, bool& ok) {
  const u16* src; int col = 2 * u;
  ok = true; ldw = 288;
  if (panel < 18) { src = ws.wq; col += 32 * panel; }
  else if (panel < 20) { src = panel == 18 ? ws.wk : ws.wv; ldw = 9; ok = u < 9; col = ok ? col : 16; }
  else if (panel < 38) { src = ws.woa; col += 32 * (panel - 20); }
  else if (panel < 94) {
    const int p = panel - 38, g = p / 7, cc = 32 * (p % 7) + col;
    src = ws.wqkv; ldw = 864; ok = cc < 216; col = ok ? (cc / 72) * 576 + g * 72 + (cc % 72) : 0;
  } else { src = ws.wob; col += 32 * (panel - 94); }
  return (const unsigned*)(src + col);
}
__device__ __forceinline__ void raw_frag(const unsigned (&r)[8], u32x4& f0, u32x4& f1) {
#pragma unroll
  for (int j = 0; j < 4; ++j) {
    f0[j] = __builtin_amdgcn_perm(r[2 * j + 1], r[2 * j], 0x05040100u);
    f1[j] = __builtin_amdgcn_perm(r[2 * j + 1], r[2 * j], 0x07060302u);
  }
}

__device__ __forceinline__ void load8(const u16* __restrict__ src, int grow0, int lane, u32x4 (&v)[9]) {
  int grow = grow0 + (lane >> 3); grow = grow < 0 ? 0 : grow;
  const u32x4* p = (const u32x4*)(src + (size_t)grow * D_) + (lane & 7);
#pragma unroll
  for (int i = 0; i < 9; ++i) v[i] = p[8 * i];
}
__device__ __forceinline__ float dot2(unsigned a, unsigned b, float c) {
  return __builtin_amdgcn_fdot2_f32_bf16(__builtin_bit_cast(bf16x2_t, a), __builtin_bit_cast(bf16x2_t, b), c, false);
}
template <int C>
__device__ __forceinline__ float dppf(float v) {
  return __int_as_float(__builtin_amdgcn_update_dpp(0, __float_as_int(v), C, 0xf, 0xf, true));
}
__device__ __forceinline__ void stat_acc(const u32x4& v, f32x4& a) {
#pragma unroll
  for (int j = 0; j < 4; j += 2) {
    a[0] = dot2(v[j], 0x3F803F80u, a[0]); a[1] = dot2(v[j + 1], 0x3F803F80u, a[1]);
    a[2] = dot2(v[j], v[j], a[2]); a[3] = dot2(v[j + 1], v[j + 1], a[3]);
  }
}
__device__ __forceinline__ void stat_fin(const f32x4& a, f32x2* st, int lr0, int lane) {
  float s = a[0] + a[1], q = a[2] + a[3];
  s += dppf<0xB1>(s); q += dppf<0xB1>(q);
  s += dppf<0x4E>(s); q += dppf<0x4E>(q);
  s += dppf<0x141>(s); q += dppf<0x141>(q);
  const float mean = s * (1.f / 576.f);
  const float var = fmaxf(q * (1.f / 576.f) - mean * mean, 0.f);
  st[lr0 + (lane >> 3)] = (f32x2){mean, rsqrtf(var + 1e-5f)};
}
__device__ __forceinline__ void stat8(const u32x4 (&v)[9], f32x2* st, int lr0, int lane) {
  f32x4 a = {0.f, 0.f, 0.f, 0.f};
#pragma unroll
  for (int i = 0; i < 9; ++i) stat_acc(v[i], a);
  stat_fin(a, st, lr0, lane);
}
typedef __attribute__((address_space(3))) volatile unsigned lds_vu32;
__device__ __forceinline__ void flag_set(unsigned* fl, int w) {
  asm volatile("" ::: "memory");
  ((lds_vu32*)fl)[w] = 1u;
  asm volatile("" ::: "memory");
}
__device__ __forceinline__ void flag_wait(unsigned* fl) {
  while (true) {
    asm volatile("" ::: "memory");
    const unsigned a = ((lds_vu32*)fl)[0], b = ((lds_vu32*)fl)[1];
    if ((a & b) != 0u) break;
    __builtin_amdgcn_s_sleep(1);
  }
  asm volatile("" ::: "memory");
}
__device__ __forceinline__ f32x2 sq2st(f32x2 sq) {
  const float mean = sq[0] * (1.f / 576.f);
  const float var = fmaxf(sq[1] * (1.f / 576.f) - mean * mean, 0.f);
  return (f32x2){mean, rsqrtf(var + 1e-5f)};
}
template <bool LN>
__device__ __forceinline__ void put8(u32x4 (&v)[9], char* lds, f32x2* st, int lr0, int lane) {
  char* d = lds + (lr0 + (lane >> 3)) * LDA + (lane & 7) * 16;
#pragma unroll
  for (int i = 0; i < 9; ++i) *(u32x4*)(d + 128 * i) = v[i];
  if (LN) stat8(v, st, lr0, lane);
}

#define MFMA16(a, b, c) __builtin_amdgcn_mfma_f32_16x16x32_bf16(a, b, c, 0, 0, 0)
#define MFMA32(a, b, c) __builtin_amdgcn_mfma_f32_32x32x16_bf16(a, b, c, 0, 0, 0)
#define SB __builtin_amdgcn_sched_barrier(0)
#define BF(x) __builtin_bit_cast(bf16x8, x)
#define BAR0() asm volatile("s_barrier" ::: "memory")
#define BAR() asm volatile("s_waitcnt lgkmcnt(0)\n\ts_barrier" ::: "memory")

template <int PRE, int NT = 2>
__device__ __forceinline__ void panel_head(const u32x4* __restrict__ p, bf16x8 (&b)[NT][KS]) {
#pragma unroll
  for (int ks = 0; ks < PRE; ++ks) {
#pragma unroll
    for (int t = 0; t < NT; ++t) b[t][ks] = __builtin_bit_cast(bf16x8, p[(t * KS + ks) * 64]);
    SB;
  }
}
template <int MT, int PRE, int NT = 2, int NG = 0, int G0 = 0>
__device__ __forceinline__ void wave_gemm(const u32x4* __restrict__ p, bf16x8 (&b)[NT][KS], const char* lds, int lane,
                                          f32x4 (&acc)[MT][NT], f32x4* gr = nullptr) {
  const char* ap = lds + (lane & 15) * LDA + (lane >> 4) * 16;
  bf16x8 a[2][MT];
#pragma unroll
  for (int n = 0; n < NG; ++n) gr[n] = (f32x4){0.f, 0.f, 0.f, 0.f};
#pragma unroll
  for (int m = 0; m < MT; ++m) {
#pragma unroll
    for (int t = 0; t < NT; ++t) acc[m][t] = (f32x4){0.f, 0.f, 0.f, 0.f};
    a[0][m] = *(const bf16x8*)(ap + m * 16 * LDA);
  }
#pragma unroll
  for (int ks = 0; ks < KS; ++ks) {
    if (ks + PRE < KS) {
#pragma unroll
      for (int t = 0; t < NT; ++t) b[t][ks + PRE] = __builtin_bit_cast(bf16x8, p[(t * KS + ks + PRE) * 64]);
    }
    if (ks + 1 < KS) {
#pragma unroll
      for (int m = 0; m < MT; ++m) a[(ks + 1) & 1][m] = *(const bf16x8*)(ap + m * 16 * LDA + (ks + 1) * 64);
    }
    SB;
#pragma unroll
    for (int m = 0; m < MT; ++m)
#pragma unroll
      for (int t = 0; t < NT; ++t) acc[m][t] = MFMA16(b[t][ks], a[ks & 1][m], acc[m][t]);
#pragma unroll
    for (int n = 0; n < NG; ++n) gr[n] = MFMA16(a[ks & 1][G0 + n], a[ks & 1][G0 + n], gr[n]);
    SB;
  }
#pragma unroll
  for (int m = 0; m < MT; ++m)
#pragma unroll
    for (int t = 0; t < NT; ++t) asm volatile("" : "+v"(acc[m][t]));
#pragma unroll
  for (int n = 0; n < NG; ++n) asm volatile("" : "+v"(gr[n]));
}
__device__ __forceinline__ void ln_fix(f32x4 (&acc)[2], const f32x4 (&cs)[2], f32x2 st, float scale) {
  const float rs = st[1] * scale;
#pragma unroll
  for (int t = 0; t < 2; ++t)
#pragma unroll
    for (int r = 0; r < 4; ++r) acc[t][r] = (acc[t][r] - st[0] * cs[t][r]) * rs;
}

constexpr int QA_LDS = 64 * LDA + 64 * 8;
__device__ __forceinline__ void qkv_store(const u32x4& v, int panel, int row, int g, u16* __restrict__ q,
                                          char* __restrict__ kv) {
      if (panel < 18) {
        wt16(q, ACT_B, (row * D_ + panel * 32 + g * 8) * 2, v);
      } else if (panel == 18) {
        char* rec = kv + (row >> 5) * KV_REC;
        if (g < 2) *(u32x4*)(rec + (row & 31) * 32 + g * 16) = v;
        else if (g == 2) *(unsigned*)(rec + KV_KHI + (row & 31) * 4) = v[0];
      } else {
        const int T = row >> 5, kk = row & 31;
        const int s = kk >> 4, h = (kk >> 2) & 1, j = ((kk >> 3) & 1) * 4 + (kk & 3);
        u16* vb = (u16*)(kv + T * KV_REC + KV_VT + ((s * 2 + h) * 19) * 16 + j * 2);
  #pragma unroll
        for (int e = 0; e < 8; ++e) {
          const int d = 8 * g + e;
          if (d < 18) vb[d * 8] = (u16)((v[e >> 1] >> ((e & 1) * 16)) & 0xffffu);
          else if (d == 18) vb[d * 8] = (u16)0x3F80;
        }
      }
}
__global__ __launch_bounds__(512) void ln_qkv_a_kernel(const u16* __restrict__ x, const u32x4* __restrict__ wp,
                                                       const float* __restrict__ csum, u16* __restrict__ q,
                                                       char* __restrict__ kv) {
  extern __shared__ __attribute__((aligned(16))) char lds[];
  const int tid = threadIdx.x, lane = tid & 63, w = tid >> 6;
  const int b = blockIdx.x;
  const int rt = 4 * (b & 7) + (b >> 3) / 7;
  const int panel = 3 * ((b >> 3) % 7) + w;
  f32x2* st = (f32x2*)(lds + 64 * LDA);
  u32x4 v8[9];
  load8(x, rt * 64 + w * 8, lane, v8);
  SB;
  BAR0();
  if (!(w < 3 && panel < 20)) {
    put8<true>(v8, lds, st, w * 8, lane);
    BAR();
    return;
  }
  const int g = lane >> 4;
  bf16x8 bw[2][KS];
  const u32x4* pp = wp + (size_t)panel * PANEL_CH + lane;
  panel_head<8>(pp, bw);
  put8<true>(v8, lds, st, w * 8, lane);
  BAR();
  f32x4 cs[2];
  cs[0] = *(const f32x4*)(csum + panel * 32 + g * 8); cs[1] = *(const f32x4*)(csum + panel * 32 + g * 8 + 4);
  f32x4 acc[4][2];
  wave_gemm<4, 8>(pp, bw, lds, lane, acc);
#pragma unroll
  for (int m = 0; m < 4; ++m) {
    const int row = rt * 64 + m * 16 + (lane & 15);
    ln_fix(acc[m], cs, st[m * 16 + (lane & 15)], panel < 18 ? 0.23570226039551584f * 1.4426950408889634f : 1.f);
    u32x4 v;
    v[0] = pk2(acc[m][0][0], acc[m][0][1]); v[1] = pk2(acc[m][0][2], acc[m][0][3]);
    v[2] = pk2(acc[m][1][0], acc[m][1][1]); v[3] = pk2(acc[m][1][2], acc[m][1][3]);
    qkv_store(v, panel, row, g, q, kv);
  }
}

typedef __attribute__((address_space(1))) const char gchar;
typedef __attribute__((address_space(1))) const unsigned guint;
template <int LDW>
__device__ __forceinline__ void raw_gemm(const char* __restrict__ src, unsigned vo0, u32x4 (&v8)[9], char* lds,
                                         f32x2* st, int w, int lane, f32x4 (&acc)[4][2], f32x4 (&cs)[2]) {
  constexpr int PRE = 7;
  unsigned vo1 = vo0 + 16 * LDW;
  asm("" : "+v"(vo1));
  unsigned r[KS][8];
#define RAWLD(K_) do { gchar* bk = (gchar*)src + (K_) * (32 * LDW * 4); asm("" : "+s"(bk)); \
    _Pragma("unroll") for (int j = 0; j < 8; ++j) \
      r[K_][j] = *(guint*)(bk + (size_t)(j < 4 ? vo0 : vo1) + (j & 3) * (LDW * 4)); } while (0)
#pragma unroll
  for (int ks = 0; ks < PRE; ++ks) {
    RAWLD(ks);
    SB;
  }
  put8<true>(v8, lds, st, w * 8, lane);
  STAMP(4, 1);
  BAR();
  STAMP(4, 2);
  const char* ap = lds + (lane & 15) * LDA + (lane >> 4) * 16;
  const u32x4 ones = {0x3F803F80u, 0x3F803F80u, 0x3F803F80u, 0x3F803F80u};
  bf16x8 a[2][4];
  u32x4 f[2][2];
  cs[0] = cs[1] = (f32x4){0.f, 0.f, 0.f, 0.f};
#pragma unroll
  for (int m = 0; m < 4; ++m) {
    acc[m][0] = acc[m][1] = (f32x4){0.f, 0.f, 0.f, 0.f};
    a[0][m] = *(const bf16x8*)(ap + m * 16 * LDA);
  }
  raw_frag(r[0], f[0][0], f[0][1]);
  SB;
#pragma unroll
  for (int ks = 0; ks < KS; ++ks) {
    if (ks + 1 < KS) {
#pragma unroll
      for (int m = 0; m < 4; ++m) a[(ks + 1) & 1][m] = *(const bf16x8*)(ap + m * 16 * LDA + (ks + 1) * 64);
    }
    if (ks + PRE < KS) RAWLD(ks + PRE);
    if (ks + 1 < KS) raw_frag(r[ks + 1], f[(ks + 1) & 1][0], f[(ks + 1) & 1][1]);
#pragma unroll
    for (int t = 0; t < 2; ++t) cs[t] = MFMA16(BF(f[ks & 1][t]), BF(ones), cs[t]);
#pragma unroll
    for (int m = 0; m < 4; ++m)
#pragma unroll
      for (int t = 0; t < 2; ++t) acc[m][t] = MFMA16(BF(f[ks & 1][t]), a[ks & 1][m], acc[m][t]);
#pragma unroll
    for (int i = 0; i < 10; ++i) {
      __builtin_amdgcn_sched_group_barrier(0x8, 1, 0);
      if (i < 4) __builtin_amdgcn_sched_group_barrier(0x100, 1, 0);
      if (i < 8) __builtin_amdgcn_sched_group_barrier(0x20, 1, 0);
      if (i >= 2) __builtin_amdgcn_sched_group_barrier(0x2, 1, 0);
    }
    SB;
  }
#undef RAWLD
#pragma unroll
  for (int m = 0; m < 4; ++m)
#pragma unroll
    for (int t = 0; t < 2; ++t) asm volatile("" : "+v"(acc[m][t]));
  asm volatile("" : "+v"(cs[0])); asm volatile("" : "+v"(cs[1]));
}
__global__ __launch_bounds__(512) void first_kernel(const u16* __restrict__ x, const u16* __restrict__ wq,
                                                    const u16* __restrict__ wk, const u16* __restrict__ wv,
                                                    const u16* __restrict__ woa, const u16* __restrict__ wqkv,
                                                    const u16* __restrict__ wob, u32x4* __restrict__ wp,
                                                    float* __restrict__ csp, u16* __restrict__ q,
                                                    char* __restrict__ kv) {
  extern __shared__ __attribute__((aligned(16))) char lds[];
  const int tid = threadIdx.x, lane = tid & 63, w = __builtin_amdgcn_readfirstlane(tid >> 6);
  const int b = blockIdx.x;
  const int rt = 4 * (b & 7) + (b >> 3) / 7;
  const int panel = 3 * ((b >> 3) % 7) + w;
  const int g = lane >> 4, u = lane & 15;
  const WSrc wsrc = {wq, wk, wv, woa, wqkv, wob};
  f32x2* st = (f32x2*)(lds + 64 * LDA);
  STAMP(4, 0);
  u32x4 v8[9];
  load8(x, rt * 64 + w * 8, lane, v8);
  SB;
  BAR0();
  if (w >= 3) {
    put8<true>(v8, lds, st, w * 8, lane);
    BAR();
    __builtin_amdgcn_s_sleep(34);
    const int rp = b >> 1, hw = w - 3, ks0 = 9 * (b & 1) + 2 * hw;
    int ldw; bool ok;
    const unsigned* p = raw_src(wsrc, rp, u, ldw, ok) + (size_t)(32 * ks0 + 8 * g) * ldw;
    unsigned r[2][8];
#pragma unroll
    for (int j = 0; j < 8; ++j) r[0][j] = p[j * ldw];
    if (hw < 4) {
#pragma unroll
      for (int j = 0; j < 8; ++j) r[1][j] = p[(32 + j) * ldw];
    }
    const unsigned fill = (rp >= 38 && (u == 12 || u == 14)) ? 0x3F80u : 0u;
    const unsigned d = rp * PANEL_CH + ((u >> 1) & 1) * (KS * 64) + 16 * g + 4 * (u >> 2) + 2 * (u & 1);
    float s0 = 0.f, s1 = 0.f;
#pragma unroll
    for (int h = 0; h < 2; ++h) {
      if (h == 0 || hw < 4) {
#pragma unroll
        for (int j = 0; j < 8; ++j) {
          r[h][j] = ok ? r[h][j] : fill;
          s0 += bflo(r[h][j]); s1 += bfhi(r[h][j]);
        }
        u32x4 f0, f1;
        raw_frag(r[h], f0, f1);
        wt16(wp, OFF_Q, (d + (ks0 + h) * 64) * 16, f0); wt16(wp, OFF_Q, (d + (ks0 + h) * 64 + 1) * 16, f1);
      }
    }
    s0 += __shfl_xor(s0, 16); s1 += __shfl_xor(s1, 16);
    s0 += __shfl_xor(s0, 32); s1 += __shfl_xor(s1, 32);
    if (lane < 16) *(f32x2*)(csp + (rp * 10 + 5 * (b & 1) + hw) * 32 + 2 * u) = (f32x2){s0, s1};
    return;
  }
  if (panel >= 20) {
    put8<true>(v8, lds, st, w * 8, lane);
    BAR();
    return;
  }
  f32x4 acc[4][2], cs[2];
  if (panel < 18) raw_gemm<288>((const char*)(wq + 32 * panel), 4 * u + g * (8 * 288 * 4), v8, lds, st, w, lane, acc, cs);
  else raw_gemm<9>((const char*)(panel == 18 ? wk : wv), 4 * (u < 9 ? u : 8) + g * (8 * 9 * 4), v8, lds, st, w, lane, acc, cs);
  STAMP(4, 3);
#pragma unroll
  for (int m = 0; m < 4; ++m) {
    const int row = rt * 64 + m * 16 + (lane & 15);
    ln_fix(acc[m], cs, st[m * 16 + (lane & 15)], panel < 18 ? 0.23570226039551584f * 1.4426950408889634f : 1.f);
    u32x4 v;
#pragma unroll
    for (int j = 0; j < 4; ++j) v[j] = pk2(acc[m][0][j], acc[m][1][j]);
    qkv_store(v, panel, row, g, q, kv);
  }
  STAMP(4, 7);
}

__global__ __launch_bounds__(512) void proj_res_kernel(const u16* __restrict__ o, const u32x4* __restrict__ wp,
                                                       const u16* __restrict__ xin, u16* __restrict__ xout,
                                                       const float* __restrict__ csp, float* __restrict__ cs) {
  extern __shared__ __attribute__((aligned(16))) char lds[];
  const int tid = threadIdx.x, lane = tid & 63, w = tid >> 6;
  const int b = blockIdx.x;
  const int rt = 4 * (b & 7) + (b >> 3) / 6, panel = 3 * ((b >> 3) % 6) + w;
  const int g = lane >> 4;
  STAMP(1, 0);
  u32x4 v8[9];
  load8(o, rt * 64 + w * 8, lane, v8);
  SB;
  BAR0();
  if (w >= 3) {
    put8<false>(v8, lds, nullptr, w * 8, lane);
    BAR();
    if (csp != nullptr && w == 7 && b < 112 && lane < 32) {
      float pv[10], sum = 0.f;
#pragma unroll
      for (int i = 0; i < 10; ++i) pv[i] = csp[(b * 10 + i) * 32 + lane];
#pragma unroll
      for (int i = 0; i < 10; ++i) sum += pv[i];
      cs[b * 32 + lane] = sum;
    }
    return;
  }
  bf16x8 bw[2][KS];
  u32x4 xr[4];
  const u32x4* pp = wp + (size_t)panel * PANEL_CH + lane;
  panel_head<8>(pp, bw);
  put8<false>(v8, lds, nullptr, w * 8, lane);
  STAMP(1, 1);
  BAR();
  STAMP(1, 2);
#pragma unroll
  for (int m = 0; m < 4; ++m) {
    const int row = rt * 64 + m * 16 + (lane & 15);
    xr[m] = *(const u32x4*)(xin + (size_t)row * D_ + panel * 32 + g * 8);
  }
  f32x4 acc[4][2];
  wave_gemm<4, 8>(pp, bw, lds, lane, acc);
  STAMP(1, 3);
#pragma unroll
  for (int m = 0; m < 4; ++m) {
    const int row = rt * 64 + m * 16 + (lane & 15);
    u32x4 v;
#pragma unroll
    for (int j = 0; j < 4; ++j)
      v[j] = pk2(bflo(xr[m][j]) + acc[m][j >> 1][(j & 1) * 2], bfhi(xr[m][j]) + acc[m][j >> 1][(j & 1) * 2 + 1]);
    wt16(xout, ACT_B, (row * D_ + panel * 32 + g * 8) * 2, v);
  }
  STAMP(1, 7);
}

constexpr int QLD = 100, VLD = 88;
constexpr int QOFF = 80 * LDA;
constexpr int VOFF = QOFF + 80 * QLD * 4;
constexpr int STOFF = VOFF + 72 * VLD * 2;
constexpr int FLOFF = STOFF + 80 * 8;
constexpr int LOC_LDS = FLOFF + 32;
__device__ __forceinline__ int segoff(int dw) { return (dw / 9) * 12 + dw % 9; }
__device__ __forceinline__ void put_vt(u16* d, unsigned v) { d[0] = (u16)v; d[VLD] = (u16)(v >> 16); }
__device__ __forceinline__ float xrow_max(float m) {
  auto a = __builtin_amdgcn_permlane16_swap(__float_as_uint(m), __float_as_uint(m), false, false);
  m = fmaxf(__uint_as_float(a[0]), __uint_as_float(a[1]));
  auto b = __builtin_amdgcn_permlane32_swap(__float_as_uint(m), __float_as_uint(m), false, false);
  return fmaxf(__uint_as_float(b[0]), __uint_as_float(b[1]));
}
template <int MT>
__device__ __forceinline__ void loc_full(const u32x4* __restrict__ pp, bf16x8 (&bw)[2][KS], const float* __restrict__ csp,
                                         char* lds, int lane, int pl) {
  constexpr int M0 = 5 - MT;
  const int g = lane >> 4;
  f32x4 cs[2];
  cs[0] = *(const f32x4*)(csp + g * 8); cs[1] = *(const f32x4*)(csp + g * 8 + 4);
  f32x4 acc[MT][2];
  f32x2* st = (f32x2*)(lds + STOFF);
  unsigned* tq = (unsigned*)(lds + QOFF) + (M0 * 16 + (lane & 15)) * QLD;
  int so[4];
#pragma unroll
  for (int j = 0; j < 4; ++j) { so[j] = segoff(pl * 16 + g * 4 + j); asm volatile("" : "+v"(so[j])); }
  wave_gemm<MT, 4>(pp, bw, lds + M0 * 16 * LDA, lane, acc);
  STAMP(2, 3); STW(6, 3, 256);
  flag_wait((unsigned*)(lds + FLOFF));
  f32x2 sv[MT];
#pragma unroll
  for (int m = 0; m < MT; ++m) sv[m] = st[(M0 + m) * 16 + (lane & 15)];
  if (pl * 32 + g * 8 < 144) {
#pragma unroll
    for (int m = 0; m < MT; ++m) {
      ln_fix(acc[m], cs, sv[m], 1.f);
      unsigned* d = tq + m * 16 * QLD;
      d[so[0]] = pk2(acc[m][0][0], acc[m][0][1]); d[so[1]] = pk2(acc[m][0][2], acc[m][0][3]);
      d[so[2]] = pk2(acc[m][1][0], acc[m][1][1]); d[so[3]] = pk2(acc[m][1][2], acc[m][1][3]);
    }
  } else {
    u16* vt = (u16*)(lds + VOFF) + (pl * 32 + g * 8 - 144) * VLD + M0 * 16 + (lane & 15);
#pragma unroll
    for (int m = 0; m < MT; ++m) {
      ln_fix(acc[m], cs, sv[m], 1.f);
      put_vt(vt + m * 16, pk2(acc[m][0][0], acc[m][0][1])); put_vt(vt + m * 16 + 2 * VLD, pk2(acc[m][0][2], acc[m][0][3]));
      put_vt(vt + m * 16 + 4 * VLD, pk2(acc[m][1][0], acc[m][1][1])); put_vt(vt + m * 16 + 6 * VLD, pk2(acc[m][1][2], acc[m][1][3]));
    }
  }
}
__global__ __launch_bounds__(512) void ln_qkv_local_kernel(const u16* __restrict__ x, const u32x4* __restrict__ wp,
                                                           const float* __restrict__ csum, u16* __restrict__ o) {
  extern __shared__ __attribute__((aligned(16))) char lds[];
  const int tid = threadIdx.x, lane = tid & 63, w = tid >> 6;
  const int b = blockIdx.x;
  const int rt = 4 * (b & 7) + (b >> 6), hg = (b >> 3) & 7;
  const int r0 = rt * 64 - 16;
  unsigned* tq = (unsigned*)(lds + QOFF);
  f32x2* st = (f32x2*)(lds + STOFF);
  STAMP(2, 0); STW(5, 0, 384); STW(6, 0, 256);
  if (w < 6) {
    const int pl = w < 2 ? w : (w < 4 ? w + 2 : w - 2);
    u32x4 v8[1][9];
    load8(x, r0 + w * 8, lane, v8[0]);
    SB;
    BAR0();
    bf16x8 bw[2][KS];
    const u32x4* pp = wp + (size_t)(hg * 7 + pl) * PANEL_CH + lane;
    panel_head<4>(pp, bw);
    put8<false>(v8[0], lds, st, w * 8, lane);
    if (tid < 8) ((unsigned*)(lds + FLOFF))[tid] = 0u;
    STAMP(2, 1);
    BAR();
    STAMP(2, 2);
    if (w < 2) loc_full<4>(pp, bw, csum + (hg * 7 + pl) * 32, lds, lane, pl);
    else loc_full<5>(pp, bw, csum + (hg * 7 + pl) * 32, lds, lane, pl);
  } else {
    const int t = w - 6, g = lane >> 4;
    u32x4 v8[2][9];
    load8(x, r0 + w * 8, lane, v8[0]);
    load8(x, r0 + w * 8 + 16, lane, v8[1]);
    SB;
    BAR0();
    bf16x8 bw[1][KS];
    const u32x4* pp = wp + (size_t)(hg * 7 + 6) * PANEL_CH + t * KS * 64 + lane;
    put8<false>(v8[0], lds, st, w * 8, lane);
    put8<false>(v8[1], lds, st, w * 8 + 16, lane);
    panel_head<8, 1>(pp, bw);
    BAR();
    STW(5, 1, 384);
    const f32x4 cs = *(const f32x4*)(csum + (hg * 7 + 6) * 32 + g * 8 + 4 * t);
    f32x4 acc[5][1];
    __builtin_amdgcn_s_setprio(3);
    float* sf = (float*)st;
    const int j = lane & 15;
    if (t == 0) {
      f32x4 gr[3];
      wave_gemm<5, 8, 1, 3, 0>(pp, bw, lds, lane, acc, gr);
#pragma unroll
      for (int n = 0; n < 3; ++n) {
        if (g == 3) sf[(n * 16 + j) * 2] = acc[n][0][0];
        if (g == (j >> 2)) sf[(n * 16 + j) * 2 + 1] = (j & 2) ? ((j & 1) ? gr[n][3] : gr[n][2]) : ((j & 1) ? gr[n][1] : gr[n][0]);
      }
    } else {
      f32x4 gr[2];
      wave_gemm<5, 8, 1, 2, 3>(pp, bw, lds, lane, acc, gr);
#pragma unroll
      for (int n = 0; n < 2; ++n) {
        if (g == 3) sf[((n + 3) * 16 + j) * 2] = acc[n + 3][0][0];
        if (g == (j >> 2)) sf[((n + 3) * 16 + j) * 2 + 1] = (j & 2) ? ((j & 1) ? gr[n][3] : gr[n][2]) : ((j & 1) ? gr[n][1] : gr[n][0]);
      }
    }
    asm volatile("" ::: "memory");
    if (lane < (t == 0 ? 48 : 32)) st[t * 48 + lane] = sq2st(st[t * 48 + lane]);
    flag_set((unsigned*)(lds + FLOFF), t);
    __builtin_amdgcn_s_setprio(0);
    STW(5, 3, 384);
    flag_wait((unsigned*)(lds + FLOFF));
    if (g < 3) {
      u16* vt = (u16*)(lds + VOFF) + (48 + g * 8 + 4 * t) * VLD + (lane & 15);
      f32x2 sv[5];
#pragma unroll
      for (int m = 0; m < 5; ++m) sv[m] = st[m * 16 + (lane & 15)];
#pragma unroll
      for (int m = 0; m < 5; ++m) {
        f32x4 a = acc[m][0];
#pragma unroll
        for (int r = 0; r < 4; ++r) a[r] = (a[r] - sv[m][0] * cs[r]) * sv[m][1];
        put_vt(vt + m * 16, pk2(a[0], a[1])); put_vt(vt + m * 16 + 2 * VLD, pk2(a[2], a[3]));
      }
    }
  }
  STW(5, 4, 384); STW(6, 4, 256);
  BAR();
  STAMP(2, 4);
  {
    const int hh = w >> 1, j = lane & 15, g = lane >> 4, par = j & 1;
    const int LB = 16 + (w & 1) * 32;
    const unsigned* fp = tq + (LB - 16 + j) * QLD + hh * 12 + 4 * (g < 2 ? g : 2);
    u32x4 kf[3], qf[2];
#pragma unroll
    for (int n = 0; n < 3; ++n) kf[n] = *(const u32x4*)(fp + n * 16 * QLD + 48);
#pragma unroll
    for (int n = 0; n < 2; ++n) qf[n] = *(const u32x4*)(fp + (n + 1) * 16 * QLD);
    const u16* vt = (const u16*)(lds + VOFF) + LB - 16 + 4 * g;
    typedef __attribute__((ext_vector_type(2))) unsigned u32x2;
    u32x2 v0[3], v1[3];
#pragma unroll
    for (int n = 0; n < 3; ++n) {
      v0[n] = *(const u32x2*)(vt + (hh * 18 + j) * VLD + n * 16);
      v1[n] = *(const u32x2*)(vt + (hh * 18 + 16 + (j & 1)) * VLD + n * 16);
      if ((j & 3) == 2) v1[n] = (u32x2){0x3F803F80u, 0x3F803F80u};
    }
#pragma unroll
    for (int n = 0; n < 3; ++n) {
      if (g >= 2) { kf[n][1] = 0u; kf[n][2] = 0u; kf[n][3] = 0u; }
      if (g == 3) kf[n][0] = 0u;
    }
#pragma unroll
    for (int n = 0; n < 2; ++n) {
      if (g >= 2) { qf[n][1] = 0u; qf[n][2] = 0u; qf[n][3] = 0u; }
      if (g == 3) qf[n][0] = 0u;
    }
    const f32x4 z = {0.f, 0.f, 0.f, 0.f};
    const unsigned pm = par ? 0xffff0000u : 0x0000ffffu;
    const int dB = j - 4 * g - par;
#pragma unroll
    for (int n = 0; n < 2; ++n) {
      const int grow = rt * 64 + LB - 16 + n * 16 + j;
      const unsigned lim = grow < 14 ? grow : 14;
      const f32x4 sa = MFMA16(BF(kf[n]), BF(qf[n]), z), sb = MFMA16(BF(kf[n + 1]), BF(qf[n]), z);
      float e[4] = {par ? sa[1] : sa[0], par ? sa[3] : sa[2], par ? sb[1] : sb[0], par ? sb[3] : sb[2]};
      if ((unsigned)(dB + 16) > lim) e[0] = -3.0e38f;
      if ((unsigned)(dB + 14) > lim) e[1] = -3.0e38f;
      if ((unsigned)dB > lim) e[2] = -3.0e38f;
      if ((unsigned)(dB - 2) > lim) e[3] = -3.0e38f;
      const float mx = xrow_max(fmaxf(fmaxf(e[0], e[1]), fmaxf(e[2], e[3])));
      constexpr float cc = 0.23570226039551584f * 1.4426950408889634f;
      u32x4 pf;
#pragma unroll
      for (int i = 0; i < 4; ++i) { const float pv = __builtin_amdgcn_exp2f((e[i] - mx) * cc); pf[i] = pk2(pv, pv) & pm; }
      const u32x4 a0 = {v0[n][0], v0[n][1], v0[n + 1][0], v0[n + 1][1]};
      const u32x4 a1 = {v1[n][0], v1[n][1], v1[n + 1][0], v1[n + 1][1]};
      const f32x4 o0 = MFMA16(BF(a0), BF(pf), z), o1 = MFMA16(BF(a1), BF(pf), z);
      const float inv = 1.f / o1[2];
      unsigned* op = (unsigned*)(o + (size_t)grow * D_ + (hg * 4 + hh) * 18);
      op[2 * g] = pk2(o0[0] * inv, o0[1] * inv);
      op[2 * g + 1] = pk2(o0[2] * inv, o0[3] * inv);
      if (g == 0) op[8] = pk2(o1[0] * inv, o1[1] * inv);
    }
  }
  STAMP(2, 7);
}

__device__ __forceinline__ float xhalf_max(float m) {
  auto rr = __builtin_amdgcn_permlane32_swap(__float_as_uint(m), __float_as_uint(m), false, false);
  return fmaxf(__uint_as_float(rr[0]), __uint_as_float(rr[1]));
}
#define EX2(x) __builtin_amdgcn_exp2f(x)
#define PMODE 1
#define SP(n) __builtin_amdgcn_s_setprio(n)
#define PRIO(it) { switch ((it) & 3) { case 0: SP(3); break; case 1: SP(2); break; case 2: SP(1); break; default: SP(0); } }
__global__ __launch_bounds__(512, 2) void attn_kernel(const u16* __restrict__ q, const u32x4* __restrict__ kv,
                                                      u16* __restrict__ o) {
  extern __shared__ __attribute__((aligned(16))) char lds[];
  const int tid = threadIdx.x, lane = tid & 63, w = tid >> 6;
  const int r = lane & 31, h = lane >> 5;
  const int qt = (blockIdx.x & 7) * 8 + (blockIdx.x >> 5), head = ((blockIdx.x >> 3) & 3) * 8 + w;
  STAMP(0, 0);
  constexpr int CH = 16 * KV_REC / 16;
  const int c4 = tid + 2048 < CH ? tid + 2048 : CH - 1;
  u32x4 tt[5];
#pragma unroll
  for (int i = 0; i < 4; ++i) tt[i] = kv[tid + i * 512];
  tt[4] = kv[c4];
  const int qrow = qt * 32 + r;
  const unsigned* qp = (const unsigned*)(q + (size_t)qrow * D_ + head * 18);
  u32x4 qlo_u, qhi_u = {0u, 0u, 0u, 0u};
#pragma unroll
  for (int i = 0; i < 4; ++i) qlo_u[i] = qp[h * 4 + i];
  if (h == 0) qhi_u[0] = qp[8];
  u32x4* l4 = (u32x4*)lds;
#define PUTCH(C) { _Pragma("unroll") for (int i = 0; i < 4; ++i) l4[(C) * CH + tid + i * 512] = tt[i]; l4[(C) * CH + c4] = tt[4]; }
  PUTCH(0);
  const bf16x8 qlo = __builtin_bit_cast(bf16x8, qlo_u), qhi = __builtin_bit_cast(bf16x8, qhi_u);
  STAMP(0, 1);
  BAR();
  STAMP(0, 2);
  const char* kloP = lds + r * 32 + h * 16;
  const char* khiP = lds + KV_KHI + r * 4;
  const int dcl = r < 19 ? r : 18;
  const char* vP = lds + KV_VT + h * 304 + dcl * 16;
  f32x16 negm, acc, sA, sB;
  bf16x8 kA, kB, vA0, vA1, vB0, vB1;
  u32x4 hA = {0u, 0u, 0u, 0u}, hB = {0u, 0u, 0u, 0u};
  u32x4 pA0, pA1, pB0 = {0u, 0u, 0u, 0u}, pB1 = {0u, 0u, 0u, 0u};
#pragma unroll
  for (int i = 0; i < 16; ++i) { negm[i] = 0.f; acc[i] = 0.f; }
  kA = *(const bf16x8*)kloP; hA[0] = *(const unsigned*)khiP;
  kB = *(const bf16x8*)(kloP + KV_REC); hB[0] = *(const unsigned*)(khiP + KV_REC);
  {
    sA = MFMA32(kA, qlo, negm);
    sA = MFMA32(BF(hA), qhi, sA);
    float tm = sA[0];
#pragma unroll
    for (int i = 1; i < 16; ++i) tm = fmaxf(tm, sA[i]);
    tm = xhalf_max(tm);
#pragma unroll
    for (int i = 0; i < 16; ++i) { negm[i] = -tm; sA[i] -= tm; }
  }
  vB0 = BF(pB0); vB1 = BF(pB0);
#define E4(SS, PD, I0, J0) { PD[J0] = pk2(EX2(SS[I0]), EX2(SS[I0 + 1])); PD[J0 + 1] = pk2(EX2(SS[I0 + 2]), EX2(SS[I0 + 3])); }
#define STEP(OFF, SC, SN, KLN, KHN, KL2, KH2, VP0, VP1, VC0, VC1, PP0, PP1, PC0, PC1) { \
    SN = MFMA32(KLN, qlo, negm); \
    KL2 = *(const bf16x8*)(kloP + (OFF) + 2 * KV_REC); KH2[0] = *(const unsigned*)(khiP + (OFF) + 2 * KV_REC); \
    SB; E4(SC, PC0, 0, 0); SB; \
    acc = MFMA32(VP0, BF(PP0), acc); \
    VC0 = *(const bf16x8*)(vP + (OFF)); VC1 = *(const bf16x8*)(vP + (OFF) + 608); \
    SB; E4(SC, PC0, 4, 2); SB; \
    SN = MFMA32(BF(KHN), qhi, SN); \
    SB; E4(SC, PC1, 8, 0); SB; \
    acc = MFMA32(VP1, BF(PP1), acc); \
    SB; E4(SC, PC1, 12, 2); SB; }
  asm volatile("" : "+v"(hA), "+v"(hB));
#define CHUNK(NEXT) { \
    _Pragma("unroll") for (int it = 0; it < 8; ++it) { \
      if (NEXT && it == 7) { PUTCH(c + 1); BAR(); } \
      PRIO(it); \
      if (NEXT && it < 2) tt[2 * it] = kn[tid + 2 * it * 512]; \
      if (NEXT && it == 2) tt[4] = kn[c4]; \
      STEP(2 * it * KV_REC, sA, sB, kB, hB, kA, hA, vB0, vB1, vA0, vA1, pB0, pB1, pA0, pA1); \
      if (NEXT && it < 2) tt[2 * it + 1] = kn[tid + (2 * it + 1) * 512]; \
      STEP((2 * it + 1) * KV_REC, sB, sA, kA, hA, kB, hB, vA0, vA1, vB0, vB1, pA0, pA1, pB0, pB1); \
    } \
    kloP += 16 * KV_REC; khiP += 16 * KV_REC; vP += 16 * KV_REC; }
  for (int c = 0; c < 3; ++c) {
    const u32x4* kn = kv + (c + 1) * CH;
    CHUNK(true);
  }
  {
    constexpr int c = 3; const u32x4* kn = kv;
    CHUNK(false);
  }
  acc = MFMA32(vB0, BF(pB0), acc);
  acc = MFMA32(vB1, BF(pB1), acc);
  STAMP(0, 3);
  {
    float chk = 0.f;
#pragma unroll
    for (int i = 0; i < 11; ++i) chk += acc[i] * 0.f;
    if (__builtin_expect(__any(!(chk == 0.f)), 0)) {
      kloP = lds + r * 32 + h * 16; khiP = lds + KV_KHI + r * 4; vP = lds + KV_VT + h * 304 + dcl * 16;
      float m = -3.0e38f;
#pragma unroll
      for (int i = 0; i < 16; ++i) { acc[i] = 0.f; negm[i] = 0.f; }
      for (int t = 0; t < 64; ++t) {
        kA = *(const bf16x8*)(kloP + t * KV_REC); hA[0] = *(const unsigned*)(khiP + t * KV_REC);
        vA0 = *(const bf16x8*)(vP + t * KV_REC); vA1 = *(const bf16x8*)(vP + t * KV_REC + 608);
        sA = MFMA32(kA, qlo, negm);
        sA = MFMA32(BF(hA), qhi, sA);
        float tm = sA[0];
#pragma unroll
        for (int i = 1; i < 16; ++i) tm = fmaxf(tm, sA[i]);
        tm = fmaxf(xhalf_max(tm), m);
        const float al = EX2(m - tm);
        m = tm;
#pragma unroll
        for (int i = 0; i < 16; ++i) { acc[i] *= al; sA[i] -= tm; }
        E4(sA, pA0, 0, 0); E4(sA, pA0, 4, 2); E4(sA, pA1, 8, 0); E4(sA, pA1, 12, 2);
        acc = MFMA32(vA0, BF(pA0), acc);
        acc = MFMA32(vA1, BF(pA1), acc);
      }
    }
  }
  auto rr = __builtin_amdgcn_permlane32_swap(__float_as_uint(acc[10]), __float_as_uint(acc[10]), false, false);
  const float l = __uint_as_float(rr[0]);
  const float inv = 1.f / l;
  unsigned* op = (unsigned*)(o + (size_t)qrow * D_ + head * 18);
  op[2 * h + 0] = pk2(acc[0] * inv, acc[1] * inv);
  op[2 * h + 1] = pk2(acc[2] * inv, acc[3] * inv);
  op[4 + 2 * h + 0] = pk2(acc[4] * inv, acc[5] * inv);
  op[4 + 2 * h + 1] = pk2(acc[6] * inv, acc[7] * inv);
  if (h == 0) op[8] = pk2(acc[8] * inv, acc[9] * inv);
  STAMP(0, 7);
}

extern "C" void kernel_launch(void* const* d_in, const int* in_sizes, int n_in, void* d_out, int out_size,
                              void* d_ws, size_t ws_size, hipStream_t stream) {
  const u16* x0 = (const u16*)d_in[0];
  const u16 *wq = (const u16*)d_in[1], *wk = (const u16*)d_in[2], *wv = (const u16*)d_in[3];
  const u16 *woa = (const u16*)d_in[4], *wqkv = (const u16*)d_in[6], *wob = (const u16*)d_in[7];
  char* ws = (char*)d_ws;
  u16* xs[5] = {(u16*)x0, (u16*)(ws + OFF_X1), (u16*)(ws + OFF_X2), (u16*)(ws + OFF_X3), (u16*)d_out};
  u16* qb = (u16*)(ws + OFF_Q);
  u16* ob = (u16*)(ws + OFF_O);
  char* kv = ws + OFF_KV;
  float* cs = (float*)(ws + OFF_CS);
  float* csp = (float*)(ws + OFF_CSP);
  (void)hipFuncSetAttribute((const void*)attn_kernel, hipFuncAttributeMaxDynamicSharedMemorySize, ATT_LDS);
  (void)hipFuncSetAttribute((const void*)first_kernel, hipFuncAttributeMaxDynamicSharedMemorySize, QA_LDS);
  (void)hipFuncSetAttribute((const void*)ln_qkv_a_kernel, hipFuncAttributeMaxDynamicSharedMemorySize, QA_LDS);
  (void)hipFuncSetAttribute((const void*)proj_res_kernel, hipFuncAttributeMaxDynamicSharedMemorySize, 64 * LDA);
  (void)hipFuncSetAttribute((const void*)ln_qkv_local_kernel, hipFuncAttributeMaxDynamicSharedMemorySize, LOC_LDS);
  for (int l = 0; l < 2; ++l) {
    if (l == 0) first_kernel<<<224, 512, QA_LDS, stream>>>(x0, wq, wk, wv, woa, wqkv, wob, (u32x4*)ws, csp, qb, kv);
    else ln_qkv_a_kernel<<<224, 512, QA_LDS, stream>>>(xs[2 * l], (const u32x4*)(ws + OFF_WQKVA), cs, qb, kv);
    attn_kernel<<<256, 512, ATT_LDS, stream>>>(qb, (const u32x4*)kv, ob);
    proj_res_kernel<<<192, 512, 64 * LDA, stream>>>(ob, (const u32x4*)(ws + OFF_WOA), xs[2 * l], xs[2 * l + 1],
                                                    l == 0 ? csp : nullptr, cs);
    ln_qkv_local_kernel<<<256, 512, LOC_LDS, stream>>>(xs[2 * l + 1], (const u32x4*)(ws + OFF_WQKVB), cs + 38 * 32, ob);
    proj_res_kernel<<<192, 512, 64 * LDA, stream>>>(ob, (const u32x4*)(ws + OFF_WOB), xs[2 * l + 1], xs[2 * l + 2],
                                                    nullptr, cs);
  }
}
```
